```python
import torch

def _softmax(x, axis=-1):
    return torch.softmax(x, dim=axis)
def _gelu(x):
    return torch.nn.functional.gelu(x, approximate="tanh")
def _silu(x):
    return torch.nn.functional.silu(x)
def _sigmoid(x):
    return torch.sigmoid(x)
def _layernorm(x, axis=-1, eps=1e-5):
    m = x.mean(dim=axis, keepdim=True)
    v = x.var(dim=axis, keepdim=True, unbiased=False)
    return (x - m) / torch.sqrt(v + eps)
def _scatter_add(tgt, idx, src, axis=0):
    return tgt.clone().index_add_(axis, idx, src)
def _segment_sum(src, seg, n):
    out = torch.zeros((n,) + src.shape[1:], dtype=src.dtype)
    return out.index_add_(0, seg, src)
def _pad(x, pw):
    flat = tuple(v for pair in reversed(pw) for v in pair)
    return torch.nn.functional.pad(x, flat)

kernel_name = 'gated_mix__gqa_block_cached__dense_activation_silu__decode'

def setup_inputs():

    _w = (lambda a: (lambda t: t.to(torch.bfloat16) if t.is_floating_point() else t)(torch.as_tensor(a)))
    def _A_setup():
        B, Sq, D = 4, 1, 640
        Hq, Hkv, Sk, Dh = 32, 32, 4096, 192
        assert Hq % Hkv == 0, (Hq, Hkv)
        return {
            "x": _w((lambda *s: torch.randn(*s, dtype=torch.bfloat16) * 0.1)(B, Sq, D)),
            "k_cache": _w((lambda *s: torch.randn(*s, dtype=torch.bfloat16) * 3.2)(B, Hkv, Sk, Dh)),
            "v_cache": _w((lambda *s: torch.randn(*s, dtype=torch.bfloat16) * 3.2)(B, Hkv, Sk, Dh)),
            "wq": _w((lambda *s: torch.randn(*s, dtype=torch.bfloat16) * 3.2)(D, Hq * Dh) * (D ** -0.5)),
            "wo": _w((lambda *s: torch.randn(*s, dtype=torch.bfloat16) * 3.2)(Hq * Dh, D) * ((Hq * Dh) ** -0.5)),
        }
    _A_in = _A_setup()

    def _B_setup():
        B, S, D, F = 4, 1, 640, 1536
        return {
            "x": _w((lambda *s: torch.randn(*s, dtype=torch.bfloat16) * 0.1)(B, S, D)),
            "w1": _w((lambda *s: torch.randn(*s, dtype=torch.bfloat16) * 3.2)(D, F) * (D ** -0.5)),
            "w2": _w((lambda *s: torch.randn(*s, dtype=torch.bfloat16) * 3.2)(F, D) * (F ** -0.5)),
        }
    _B_in = _B_setup()

    return {
        "A_x": _A_in["x"],
        "A_k_cache": _A_in["k_cache"],
        "A_v_cache": _A_in["v_cache"],
        "A_wq": _A_in["wq"],
        "A_wo": _A_in["wo"],
        "B_x": _B_in["x"],
        "B_w1": _B_in["w1"],
        "B_w2": _B_in["w2"],
        "gate_w": _w((lambda *s: torch.randn(*s, dtype=torch.bfloat16) * 0.1)(640) * 16.0),
    }

def reference(A_x, A_k_cache, A_v_cache, A_wq, A_wo, B_x, B_w1, B_w2, gate_w):
    def _A_ref(x, k_cache, v_cache, wq, wo):
        B, Sq, D = x.shape
        Hq, Dh = 32, 192
        Hkv = k_cache.shape[1]
        rep = Hq // Hkv
        q = (lambda x, *d: x.permute(*d))((x @ wq).reshape(B, Sq, Hq, Dh), 0, 2, 1, 3)
        k = (lambda x, n, axis: torch.repeat_interleave(x, n, dim=axis))(k_cache, rep, axis=1)
        v = (lambda x, n, axis: torch.repeat_interleave(x, n, dim=axis))(v_cache, rep, axis=1)
        scores = torch.einsum("bhqd,bhkd->bhqk", q, k) * (Dh ** -0.5)
        p = _softmax(scores, axis=-1)
        o = torch.einsum("bhqk,bhkd->bhqd", p, v)
        return (lambda x, *d: x.permute(*d))(o, 0, 2, 1, 3).reshape(B, Sq, Hq * Dh) @ wo

    def _B_ref(x, w1, w2):
        h = torch.einsum("bsd,df->bsf", x, w1)
        h = _silu(h)
        out = torch.einsum("bsf,fd->bsd", h, w2)
        return out

    x = A_x
    g = _sigmoid((x * gate_w).sum(-1, keepdims=True))
    a = _A_ref(x, A_k_cache, A_v_cache, A_wq, A_wo)
    b = _B_ref(x, B_w1, B_w2)
    return g * a + (1 - g) * b
```

```cpp
#include <hip/hip_runtime.h>
#include <hip/hip_bf16.h>
#include <cstdint>

namespace {

constexpr int NB = 4, DM = 640, NH = 32, SK = 4096, DH = 192, FF = 1536;
constexpr int NWG = 256, NSW = 8, NAW = 4, NTHR = (NSW + NAW) * 64;
constexpr int ROWB = DH * 2;
constexpr int TILEB = 8 * ROWB;
constexpr int KEYS_WG = SK / 2;
constexpr int KEYS_W = KEYS_WG / NSW;
#define WIN_ 8
#define AUXPRIO
constexpr int WIN = WIN_;
constexpr int AWIN = 5;
constexpr int KTOT = DH + 48;
constexpr int NCOL = 80;

constexpr int WS_EPOCH = 0;
constexpr int WS_S1 = 4096;
constexpr int S1_STRIDE = 256 * 8;
constexpr int WS_S2 = WS_S1 + NWG * S1_STRIDE;
constexpr int S2_STRIDE = 320 * 8;
constexpr int WS_STAMP = WS_S2 + NWG * S2_STRIDE;
constexpr int WS_BYTES = WS_STAMP + NWG * 16 * 8;

typedef unsigned u32x4 __attribute__((ext_vector_type(4)));
typedef unsigned u32x2 __attribute__((ext_vector_type(2)));
typedef float f32x4 __attribute__((ext_vector_type(4)));
typedef float f32x2 __attribute__((ext_vector_type(2)));
typedef __bf16 bf16x2 __attribute__((ext_vector_type(2)));
typedef __amdgpu_buffer_rsrc_t rsrc_t;
#define GAS __attribute__((address_space(1)))
#define RLX_AGENT __ATOMIC_RELAXED, __HIP_MEMORY_SCOPE_AGENT
constexpr int AUX_NT = 2, AUX_SC1 = 16;
constexpr unsigned SPIN_CAP = 1u << 18;

__device__ __forceinline__ rsrc_t mk_rsrc(const void* p, unsigned bytes) {
  return __builtin_amdgcn_make_buffer_rsrc(const_cast<void*>(p), (short)0, (int)bytes, 0x00020000);
}
__device__ __forceinline__ float bflo(unsigned u) { return __builtin_bit_cast(float, u << 16); }
__device__ __forceinline__ float bfhi(unsigned u) { return __builtin_bit_cast(float, u & 0xffff0000u); }
__device__ __forceinline__ unsigned cvtpk(float lo, float hi) {
  unsigned r; asm("v_cvt_pk_bf16_f32 %0, %1, %2" : "=v"(r) : "v"(lo), "v"(hi)); return r;
}
__device__ __forceinline__ float rbf(float x) { return bflo(cvtpk(x, x)); }
__device__ __forceinline__ float dot2(unsigned k, unsigned q, float acc) {
  return __builtin_amdgcn_fdot2_f32_bf16(__builtin_bit_cast(bf16x2, k), __builtin_bit_cast(bf16x2, q), acc, false);
}
template <int CTRL> __device__ __forceinline__ float dpp(float x) {
  return __builtin_bit_cast(float, __builtin_amdgcn_mov_dpp(__builtin_bit_cast(int, x), CTRL, 0xf, 0xf, true));
}
constexpr int XOR1 = 0xB1, XOR2 = 0x4E, XOR7 = 0x141, ROR8 = 0x128;
__device__ __forceinline__ float swap32_add(float a, float b) {
  auto s = __builtin_amdgcn_permlane32_swap(__float_as_uint(a), __float_as_uint(b), false, false);
  return __uint_as_float(s[0]) + __uint_as_float(s[1]);
}
__device__ __forceinline__ float swap16_add(float a, float b) {
  auto s = __builtin_amdgcn_permlane16_swap(__float_as_uint(a), __float_as_uint(b), false, false);
  return __uint_as_float(s[0]) + __uint_as_float(s[1]);
}
__device__ __forceinline__ float wave_max(float x) {
  x = fmaxf(x, dpp<XOR1>(x)); x = fmaxf(x, dpp<XOR2>(x)); x = fmaxf(x, dpp<XOR7>(x)); x = fmaxf(x, dpp<ROR8>(x));
  auto s = __builtin_amdgcn_permlane16_swap(__float_as_uint(x), __float_as_uint(x), false, false);
  x = fmaxf(__uint_as_float(s[0]), __uint_as_float(s[1]));
  auto t = __builtin_amdgcn_permlane32_swap(__float_as_uint(x), __float_as_uint(x), false, false);
  return fmaxf(__uint_as_float(t[0]), __uint_as_float(t[1]));
}
__device__ __forceinline__ float wave_sum(float x) {
  x += dpp<XOR1>(x); x += dpp<XOR2>(x); x += dpp<XOR7>(x); x += dpp<ROR8>(x);
  x = swap16_add(x, x);
  return swap32_add(x, x);
}
template <int N> __device__ __forceinline__ void tr_reduce(float (&v)[N], int lane) {
#pragma unroll
  for (int i = 0; i < N / 2; ++i) v[i] = swap32_add(v[i], v[i + N / 2]);
#pragma unroll
  for (int i = 0; i < N / 4; ++i) v[i] = swap16_add(v[i], v[i + N / 4]);
  const bool b3 = (lane & 8) != 0;
#pragma unroll
  for (int i = 0; i < N / 8; ++i) {
    const float keep = b3 ? v[i + N / 8] : v[i], send = b3 ? v[i] : v[i + N / 8];
    v[i] = keep + dpp<ROR8>(send);
  }
}
#define BAR() asm volatile("s_waitcnt lgkmcnt(0)\n\ts_barrier" ::: "memory")
#define SCHED_FENCE() __builtin_amdgcn_sched_barrier(0)

template <int AUX> __device__ __forceinline__ void ld_tile(u32x4 (&t)[3], rsrc_t rs, int voff, int soff) {
  t[0] = __builtin_amdgcn_raw_buffer_load_b128(rs, voff, soff, AUX);
  t[1] = __builtin_amdgcn_raw_buffer_load_b128(rs, voff + 128, soff, AUX);
  t[2] = __builtin_amdgcn_raw_buffer_load_b128(rs, voff + 256, soff, AUX);
}
__device__ __forceinline__ void fma_tile(float (&acc)[24], const u32x4 (&t)[3], float p) {
#pragma unroll
  for (int s = 0; s < 3; ++s)
#pragma unroll
    for (int k = 0; k < 4; ++k) {
      acc[s * 8 + 2 * k] = fmaf(p, bflo(t[s][k]), acc[s * 8 + 2 * k]);
      acc[s * 8 + 2 * k + 1] = fmaf(p, bfhi(t[s][k]), acc[s * 8 + 2 * k + 1]);
    }
}
__device__ __forceinline__ void pin_acc(float (&a)[24]) {
  asm volatile("" : "+v"(a[0]), "+v"(a[1]), "+v"(a[2]), "+v"(a[3]), "+v"(a[4]), "+v"(a[5]), "+v"(a[6]), "+v"(a[7]),
               "+v"(a[8]), "+v"(a[9]), "+v"(a[10]), "+v"(a[11]), "+v"(a[12]), "+v"(a[13]), "+v"(a[14]), "+v"(a[15]),
               "+v"(a[16]), "+v"(a[17]), "+v"(a[18]), "+v"(a[19]), "+v"(a[20]), "+v"(a[21]), "+v"(a[22]), "+v"(a[23])
               :: "memory");
}
__device__ __forceinline__ void st_granule(rsrc_t rs, int off, unsigned tag, float v) {
  u32x2 g = {__float_as_uint(v), tag};
  __builtin_amdgcn_raw_buffer_store_b64(g, rs, off, 0, AUX_SC1);
}

struct __attribute__((aligned(16))) Smem {
  float xs[NB * DM];
  float pbuf[NSW * 256];
  float mrg[NSW * DH];
  float ml[NSW * 2];
  float qred[NAW * DH];
  unsigned qpk[DH / 2];
  float hred[NAW * NB * 64];
  float gred[NAW * NB];
  unsigned tag[4];
  float act[KTOT * NB];
  unsigned short wsl[KTOT * NCOL];
  float zred[30 * NB * NCOL];
  float fin[320];
};

#define STAMP(i) do {} while (0)

__global__ void __launch_bounds__(NTHR, 3)
fused_kernel(const unsigned short* __restrict__ x, const unsigned short* __restrict__ kc, const unsigned short* __restrict__ vc,
             const unsigned short* __restrict__ wq, const unsigned short* __restrict__ wo, const unsigned short* __restrict__ w1,
             const unsigned short* __restrict__ w2, const unsigned short* __restrict__ gw, unsigned short* __restrict__ out,
             unsigned char* __restrict__ ws) {
  __shared__ Smem sm;
  const int tid = threadIdx.x, lane = tid & 63, wave = __builtin_amdgcn_readfirstlane(tid >> 6);
  const int wg = blockIdx.x, xcd = wg & 7, idx = wg >> 3;
  const int h = xcd * 4 + (idx >> 3), j = idx & 7, b = j >> 1, sp = j & 1;
  const int r = lane >> 3, c = lane & 7;
  const rsrc_t rsW = mk_rsrc(ws, WS_BYTES);
  bool sec_aux = true; (void)sec_aux;
  STAMP(0);

  float acc[24];
#pragma unroll
  for (int i = 0; i < 24; ++i) acc[i] = 0.f;

  if (wave < NSW) {
    const rsrc_t rsK = mk_rsrc(kc, NB * NH * SK * ROWB), rsV = mk_rsrc(vc, NB * NH * SK * ROWB), rs0 = mk_rsrc(vc, 0);
    const int voff = r * ROWB + c * 16;
    const int base = ((b * NH + h) * SK + sp * KEYS_WG + wave * KEYS_W) * ROWB;
    u32x4 buf[WIN][3];
#pragma unroll
    for (int u = 0; u < WIN; ++u) ld_tile<AUX_NT>(buf[u], rsK, voff, base + u * TILEB);
    SCHED_FENCE();
    BAR();
    BAR();
    STAMP(1);
    u32x4 q[3];
#pragma unroll
    for (int s = 0; s < 3; ++s) q[s] = *reinterpret_cast<const u32x4*>(&sm.qpk[s * 32 + c * 4]);
    float* pw = &sm.pbuf[wave * 256 + r * 32];
#pragma unroll 1
    for (int it = 0; it < 4; ++it) {
      const rsrc_t rn = it < 3 ? rsK : rsV;
      const int cb = base + it * 8 * TILEB, nb = it < 3 ? cb + 8 * TILEB : base;
      float sc = 0.f;
#pragma unroll
      for (int u = 0; u < 8; ++u) {
        float d0 = 0.f, d1 = 0.f, d2 = 0.f;
#pragma unroll
        for (int k = 0; k < 4; ++k) { d0 = dot2(buf[u % WIN][0][k], q[0][k], d0); d1 = dot2(buf[u % WIN][1][k], q[1][k], d1); d2 = dot2(buf[u % WIN][2][k], q[2][k], d2); }
        SCHED_FENCE();
        if (u + WIN < 8) ld_tile<AUX_NT>(buf[u % WIN], rsK, voff, cb + (u + WIN) * TILEB);
        else ld_tile<AUX_NT>(buf[u % WIN], rn, voff, nb + (u + WIN - 8) * TILEB);
        SCHED_FENCE();
        float d = (d0 + d1) + d2;
        d += dpp<XOR1>(d); d += dpp<XOR2>(d); d += dpp<XOR7>(d);
        sc = (c == u) ? d : sc;
      }
      pw[it * 8 + c] = sc;
    }
    STAMP(2);
    constexpr float C2 = 0.07216878364870322f * 1.4426950408889634f;
    float s4[4], m = -INFINITY;
#pragma unroll
    for (int it = 0; it < 4; ++it) { s4[it] = pw[it * 8 + c] * C2; m = fmaxf(m, s4[it]); }
    m = wave_max(m);
    float l = 0.f;
#pragma unroll
    for (int it = 0; it < 4; ++it) { const float p = __builtin_amdgcn_exp2f(s4[it] - m); l += p; pw[it * 8 + c] = p; }
    l = wave_sum(l);
#pragma unroll 1
    for (int it = 0; it < 4; ++it) {
      const rsrc_t rn = it < 3 ? rsV : rs0;
      const int cb = base + it * 8 * TILEB;
      const f32x4 pa = *reinterpret_cast<const f32x4*>(pw + it * 8), pb = *reinterpret_cast<const f32x4*>(pw + it * 8 + 4);
#pragma unroll
      for (int u = 0; u < 8; ++u) {
        fma_tile(acc, buf[u % WIN], u < 4 ? pa[u & 3] : pb[u & 3]);
        pin_acc(acc);
        SCHED_FENCE();
        ld_tile<AUX_NT>(buf[u % WIN], u + WIN < 8 ? rsV : rn, voff, cb + (u + WIN) * TILEB);
        SCHED_FENCE();
      }
    }
    STAMP(3);
    tr_reduce<24>(acc, lane);
    const int a0 = 3 * ((lane >> 3) & 1) + 6 * ((lane >> 4) & 1) + 12 * (lane >> 5);
#pragma unroll
    for (int i = 0; i < 3; ++i) { const int a = a0 + i; sm.mrg[wave * DH + (a >> 3) * 64 + c * 8 + (a & 7)] = acc[i]; }
    if (lane == 0) { sm.ml[wave * 2] = m; sm.ml[wave * 2 + 1] = l; }
  } else {
    __builtin_amdgcn_s_setprio(3);
    const int v = wave - NSW, row0 = v * 160;
    const rsrc_t rsQ = mk_rsrc(wq, DM * NH * DH * 2);
    const int voff = (row0 + r) * (NH * DH * 2) + h * ROWB + c * 16;
    constexpr int QT = 8 * NH * DH * 2;
    const int rot = (j * 5) >> 1;
    u32x4 buf[AWIN][3];
#pragma unroll
    for (int u = 0; u < AWIN; ++u) { int t = rot + u; t = t >= 20 ? t - 20 : t; ld_tile<0>(buf[u], rsQ, voff, t * QT); }
    SCHED_FENCE();
    {
      const unsigned* x32 = reinterpret_cast<const unsigned*>(x);
      const unsigned* g32 = reinterpret_cast<const unsigned*>(gw);
      unsigned xv[5], gv[5];
#pragma unroll
      for (int i = 0; i < 5; ++i) { const int e = i * 64 + lane, bb = e / 80, dd = e - bb * 80; xv[i] = x32[bb * (DM / 2) + row0 / 2 + dd]; gv[i] = g32[row0 / 2 + dd]; }
      float gp[4] = {0.f, 0.f, 0.f, 0.f};
#pragma unroll
      for (int i = 0; i < 5; ++i) {
        const int e = i * 64 + lane, bb = e / 80, dd = e - bb * 80;
        const float x0 = bflo(xv[i]), x1 = bfhi(xv[i]);
        *reinterpret_cast<f32x2*>(&sm.xs[bb * DM + row0 + 2 * dd]) = f32x2{x0, x1};
        const float pr = rbf(x0 * bflo(gv[i])) + rbf(x1 * bfhi(gv[i]));
#pragma unroll
        for (int bq = 0; bq < 4; ++bq) gp[bq] += (bb == bq) ? pr : 0.f;
      }
#pragma unroll
      for (int bq = 0; bq < 4; ++bq) { const float s = wave_sum(gp[bq]); if (lane == 0) sm.gred[v * 4 + bq] = s; }
    }
    const float* xb = &sm.xs[b * DM + row0 + r];
#pragma unroll 1
    for (int it = 0; it < 20 / AWIN; ++it) {
      const rsrc_t rn = it < 20 / AWIN - 1 ? rsQ : mk_rsrc(wq, 0);
#pragma unroll
      for (int u = 0; u < AWIN; ++u) {
        int t = rot + it * AWIN + u; t = t >= 20 ? t - 20 : t;
        int tn = t + AWIN; tn = tn >= 20 ? tn - 20 : tn;
        fma_tile(acc, buf[u], xb[t * 8]);
        pin_acc(acc);
        SCHED_FENCE();
        ld_tile<0>(buf[u], rn, voff, tn * QT);
        SCHED_FENCE();
      }
    }
    tr_reduce<24>(acc, lane);
    const int a0 = 3 * ((lane >> 3) & 1) + 6 * ((lane >> 4) & 1) + 12 * (lane >> 5);
#pragma unroll
    for (int i = 0; i < 3; ++i) { const int a = a0 + i; sm.qred[v * DH + (a >> 3) * 64 + c * 8 + (a & 7)] = acc[i]; }
    BAR();
    if (tid - NSW * 64 < DH / 2) {
      const int d = (tid - NSW * 64) * 2;
      float q0 = 0.f, q1 = 0.f;
#pragma unroll
      for (int w = 0; w < NAW; ++w) { const f32x2 t = *reinterpret_cast<const f32x2*>(&sm.qred[w * DH + d]); q0 += t[0]; q1 += t[1]; }
      sm.qpk[d >> 1] = cvtpk(q0, q1);
    }
    BAR();
    STAMP(1);
    const unsigned tag = __hip_atomic_load((GAS unsigned*)(ws + WS_EPOCH), RLX_AGENT) + 1u;
    {
      const rsrc_t rs1 = mk_rsrc(w1, DM * FF * 2);
      const int vo = c < 6 ? (row0 + r) * (FF * 2) + h * 96 + c * 16 : 0x7ffffff0;
      u32x4 wb[20];
#pragma unroll
      for (int t = 0; t < 20; ++t) wb[t] = __builtin_amdgcn_raw_buffer_load_b128(rs1, vo, t * 8 * FF * 2, 0);
      SCHED_FENCE();
      float ha[32];
#pragma unroll
      for (int i = 0; i < 32; ++i) ha[i] = 0.f;
#pragma unroll
      for (int t = 0; t < 20; ++t)
#pragma unroll
        for (int bb = 0; bb < 4; ++bb) {
          const float xv = sm.xs[bb * DM + row0 + t * 8 + r];
#pragma unroll
          for (int k = 0; k < 4; ++k) {
            ha[bb * 8 + 2 * k] = fmaf(xv, bflo(wb[t][k]), ha[bb * 8 + 2 * k]);
            ha[bb * 8 + 2 * k + 1] = fmaf(xv, bfhi(wb[t][k]), ha[bb * 8 + 2 * k + 1]);
          }
        }
      tr_reduce<32>(ha, lane);
      const int i0 = 4 * ((lane >> 3) & 1) + 8 * ((lane >> 4) & 1) + 16 * (lane >> 5);
#pragma unroll
      for (int i = 0; i < 4; ++i) { const int a = i0 + i; sm.hred[(v * NB + (a >> 3)) * 64 + c * 8 + (a & 7)] = ha[i]; }
    }
    STAMP(3);
    {
      const int at = tid - NSW * 64;
      u32x4 wv[10];
#pragma unroll
      for (int i = 0; i < 10; ++i) {
        const int e = i * 256 + at, row = e / 10, ch = e - row * 10;
        const unsigned short* src = row < DH ? wo + (size_t)(h * DH + row) * DM : w2 + (size_t)(h * 48 + row - DH) * DM;
        wv[i] = u32x4{0u, 0u, 0u, 0u};
        if (e < KTOT * 10) wv[i] = *reinterpret_cast<const u32x4*>(src + j * NCOL + ch * 8);
      }
#pragma unroll
      for (int i = 0; i < 10; ++i) { const int e = i * 256 + at; if (e < KTOT * 10) *reinterpret_cast<u32x4*>(&sm.wsl[e * 8]) = wv[i]; }
      STAMP(5);
      if (at == 0) sm.tag[0] = tag;
      if (at < 194) st_granule(rsW, WS_S1 + wg * S1_STRIDE + at * 8, tag - 1u, 0.f);
      for (int e = at; e < 320; e += 256) {
        const int bb = e / 80, col = e - bb * 80, f = (j * 4 + bb) * 8 + col / 10;
        st_granule(rsW, WS_S2 + f * S2_STRIDE + (h * 10 + col % 10) * 8, tag - 1u, 0.f);
      }
      asm volatile("s_waitcnt vmcnt(0)" ::: "memory");
    }
    STAMP(2);
  }
  BAR();
  sec_aux = false;
  STAMP(4);
  const unsigned tag = sm.tag[0];
  if (tid < 194) {
    float M = sm.ml[0];
#pragma unroll
    for (int w = 1; w < NSW; ++w) M = fmaxf(M, sm.ml[2 * w]);
    float o = 0.f;
#pragma unroll
    for (int w = 0; w < NSW; ++w) {
      const float e = __builtin_amdgcn_exp2f(sm.ml[2 * w] - M);
      o = fmaf(e, tid < DH ? sm.mrg[w * DH + tid] : sm.ml[2 * w + 1], o);
    }
    st_granule(rsW, WS_S1 + wg * S1_STRIDE + tid * 8, tag, tid == 192 ? M : o);
  }
  float gate;
  {
    const int bb = tid < 768 ? tid / DH : 0;
    float gs = (sm.gred[bb] + sm.gred[4 + bb]) + (sm.gred[8 + bb] + sm.gred[12 + bb]);
    gs = rbf(gs);
    gate = rbf(1.f / (1.f + __expf(-gs)));
    const int d = tid - bb * DH;
    if (d < 48) {
      float hs = (sm.hred[(0 * NB + bb) * 64 + d] + sm.hred[(1 * NB + bb) * 64 + d]) + (sm.hred[(2 * NB + bb) * 64 + d] + sm.hred[(3 * NB + bb) * 64 + d]);
      hs = rbf(hs);
      const float si = rbf(hs / (1.f + __expf(-hs)));
      sm.act[(DH + d) * NB + bb] = si * rbf(1.f - gate);
    }
  }
  bool okall = true;
  {
    const int bb = tid / DH, d = tid - bb * DH;
    const int p0 = xcd + 8 * ((h & 3) * 8 + bb * 2), p1 = p0 + 8;
    const int o0 = WS_S1 + p0 * S1_STRIDE, o1 = WS_S1 + p1 * S1_STRIDE;
    u32x2 g0, g1; u32x4 m0, m1;
    for (unsigned spin = 0;; ++spin) {
      asm volatile("" ::: "memory");
      g0 = __builtin_amdgcn_raw_buffer_load_b64(rsW, o0 + d * 8, 0, AUX_SC1);
      g1 = __builtin_amdgcn_raw_buffer_load_b64(rsW, o1 + d * 8, 0, AUX_SC1);
      m0 = __builtin_amdgcn_raw_buffer_load_b128(rsW, o0 + 192 * 8, 0, AUX_SC1);
      m1 = __builtin_amdgcn_raw_buffer_load_b128(rsW, o1 + 192 * 8, 0, AUX_SC1);
      const bool ok = g0[1] == tag && g1[1] == tag && m0[1] == tag && m0[3] == tag && m1[1] == tag && m1[3] == tag;
      if (__all(ok)) break;
      if (spin > SPIN_CAP) { okall = false; break; }
      __builtin_amdgcn_s_sleep(1);
    }
    const float M0 = __uint_as_float(m0[0]), L0 = __uint_as_float(m0[2]), M1 = __uint_as_float(m1[0]), L1 = __uint_as_float(m1[2]);
    const float M = fmaxf(M0, M1), e0 = __builtin_amdgcn_exp2f(M0 - M), e1 = __builtin_amdgcn_exp2f(M1 - M);
    const float o = (e0 * __uint_as_float(g0[0]) + e1 * __uint_as_float(g1[0])) / (e0 * L0 + e1 * L1);
    sm.act[d * NB + bb] = okall ? rbf(o) * gate : __builtin_nanf("");
  }
  STAMP(5);
  BAR();
  if (tid < 600) {
    const int kg = tid / 20, cq = tid - kg * 20;
    float z[4][4];
#pragma unroll
    for (int i = 0; i < 4; ++i)
#pragma unroll
      for (int k = 0; k < 4; ++k) z[i][k] = 0.f;
#pragma unroll
    for (int rr = 0; rr < 8; ++rr) {
      const int row = kg * 8 + rr;
      const u32x2 wv = *reinterpret_cast<const u32x2*>(&sm.wsl[row * NCOL + cq * 4]);
      const f32x4 a = *reinterpret_cast<const f32x4*>(&sm.act[row * NB]);
      const float w0 = bflo(wv[0]), w1f = bfhi(wv[0]), w2f = bflo(wv[1]), w3 = bfhi(wv[1]);
#pragma unroll
      for (int i = 0; i < 4; ++i) { z[i][0] = fmaf(a[i], w0, z[i][0]); z[i][1] = fmaf(a[i], w1f, z[i][1]); z[i][2] = fmaf(a[i], w2f, z[i][2]); z[i][3] = fmaf(a[i], w3, z[i][3]); }
    }
#pragma unroll
    for (int i = 0; i < 4; ++i) *reinterpret_cast<f32x4*>(&sm.zred[kg * 320 + i * NCOL + cq * 4]) = f32x4{z[i][0], z[i][1], z[i][2], z[i][3]};
  }
  BAR();
  if (tid < 320) {
    float z = 0.f;
#pragma unroll
    for (int kg = 0; kg < 30; ++kg) z += sm.zred[kg * 320 + tid];
    const int bb = tid / 80, col = tid - bb * 80, f = (j * 4 + bb) * 8 + col / 10;
    st_granule(rsW, WS_S2 + f * S2_STRIDE + (h * 10 + col % 10) * 8, tag, z);
    u32x2 g;
    bool ok2 = true;
    for (unsigned spin = 0;; ++spin) {
      asm volatile("" ::: "memory");
      g = __builtin_amdgcn_raw_buffer_load_b64(rsW, WS_S2 + wg * S2_STRIDE + tid * 8, 0, AUX_SC1);
      if (__all(g[1] == tag)) break;
      if (spin > SPIN_CAP) { ok2 = false; break; }
      __builtin_amdgcn_s_sleep(1);
    }
    sm.fin[tid] = ok2 ? __uint_as_float(g[0]) : __builtin_nanf("");
  }
  STAMP(6);
  BAR();
  if (tid < 10) {
    float z = 0.f;
#pragma unroll
    for (int hh = 0; hh < NH; ++hh) z += sm.fin[hh * 10 + tid];
    const int jj = wg >> 5, bb = (wg >> 3) & 3, sub = wg & 7;
    out[bb * DM + jj * NCOL + sub * 10 + tid] = (unsigned short)(cvtpk(z, z) & 0xffffu);
    if (wg == 0 && tid == 0) __hip_atomic_store((GAS unsigned*)(ws + WS_EPOCH), tag, RLX_AGENT);
  }
  STAMP(7);
}

}

extern "C" void kernel_launch(void* const* d_in, const int* in_sizes, int n_in, void* d_out, int out_size, void* d_ws,
                              size_t ws_size, hipStream_t stream) {
  (void)in_sizes; (void)n_in; (void)out_size; (void)ws_size;
  const unsigned short* ax = (const unsigned short*)d_in[0];
  const unsigned short* kc = (const unsigned short*)d_in[1];
  const unsigned short* vc = (const unsigned short*)d_in[2];
  const unsigned short* wq = (const unsigned short*)d_in[3];
  const unsigned short* wo = (const unsigned short*)d_in[4];
  const unsigned short* w1 = (const unsigned short*)d_in[6];
  const unsigned short* w2 = (const unsigned short*)d_in[7];
  const unsigned short* gw = (const unsigned short*)d_in[8];
  fused_kernel<<<NWG, NTHR, 0, stream>>>(ax, kc, vc, wq, wo, w1, w2, gw, (unsigned short*)d_out, (unsigned char*)d_ws);
}
```

```cpp
#include <hip/hip_runtime.h>
#include <hip/hip_bf16.h>
#include <cstdint>

namespace {

constexpr int NB = 4, DM = 640, NH = 32, SK = 4096, DH = 192, FF = 1536;
constexpr int NWG = 256, NSW = 8, NAW = 4, NTHR = (NSW + NAW) * 64;
constexpr int ROWB = DH * 2;
constexpr int TILEB = 8 * ROWB;
constexpr int KEYS_WG = SK / 2;
constexpr int KEYS_W = KEYS_WG / NSW;
#define POLLDELAY 60
#define DT_ 0
constexpr unsigned DT = DT_;
constexpr int LT = 4;
constexpr int WIN = 8;
constexpr int KTOT = DH + 48;
constexpr int NCOL = 80;

constexpr int WS_EPOCH = 0;
constexpr int WS_S1 = 4096;
constexpr int S1_STRIDE = 256 * 8;
constexpr int WS_S2 = WS_S1 + NWG * S1_STRIDE;
constexpr int S2_STRIDE = 320 * 8;
constexpr int QP = (DH + 48) / 2;
constexpr int WS_Q = WS_S2 + NWG * S2_STRIDE;
constexpr int WS_STAMP = WS_Q + NH * NB * 8 * QP * 16;
constexpr int WS_BYTES = WS_STAMP + NWG * 16 * 8;

typedef unsigned u32x4 __attribute__((ext_vector_type(4)));
typedef unsigned u32x2 __attribute__((ext_vector_type(2)));
typedef float f32x4 __attribute__((ext_vector_type(4)));
typedef float f32x2 __attribute__((ext_vector_type(2)));
typedef __bf16 bf16x2 __attribute__((ext_vector_type(2)));
typedef __amdgpu_buffer_rsrc_t rsrc_t;
#define GAS __attribute__((address_space(1)))
#define RLX_AGENT __ATOMIC_RELAXED, __HIP_MEMORY_SCOPE_AGENT
constexpr int AUX_NT = 2, AUX_SC1 = 16;
constexpr unsigned SPIN_CAP = 1u << 18;

__device__ __forceinline__ rsrc_t mk_rsrc(const void* p, unsigned bytes) {
  return __builtin_amdgcn_make_buffer_rsrc(const_cast<void*>(p), (short)0, (int)bytes, 0x00020000);
}
__device__ __forceinline__ float bflo(unsigned u) { return __builtin_bit_cast(float, u << 16); }
__device__ __forceinline__ float bfhi(unsigned u) { return __builtin_bit_cast(float, u & 0xffff0000u); }
__device__ __forceinline__ unsigned cvtpk(float lo, float hi) {
  unsigned r; asm("v_cvt_pk_bf16_f32 %0, %1, %2" : "=v"(r) : "v"(lo), "v"(hi)); return r;
}
__device__ __forceinline__ float rbf(float x) { return bflo(cvtpk(x, x)); }
__device__ __forceinline__ float dot2(unsigned k, unsigned q, float acc) {
  return __builtin_amdgcn_fdot2_f32_bf16(__builtin_bit_cast(bf16x2, k), __builtin_bit_cast(bf16x2, q), acc, false);
}
template <int CTRL> __device__ __forceinline__ float dpp(float x) {
  return __builtin_bit_cast(float, __builtin_amdgcn_mov_dpp(__builtin_bit_cast(int, x), CTRL, 0xf, 0xf, true));
}
constexpr int XOR1 = 0xB1, XOR2 = 0x4E, XOR7 = 0x141, ROR8 = 0x128;
__device__ __forceinline__ float swap32_add(float a, float b) {
  auto s = __builtin_amdgcn_permlane32_swap(__float_as_uint(a), __float_as_uint(b), false, false);
  return __uint_as_float(s[0]) + __uint_as_float(s[1]);
}
__device__ __forceinline__ float swap16_add(float a, float b) {
  auto s = __builtin_amdgcn_permlane16_swap(__float_as_uint(a), __float_as_uint(b), false, false);
  return __uint_as_float(s[0]) + __uint_as_float(s[1]);
}
__device__ __forceinline__ float wave_max(float x) {
  x = fmaxf(x, dpp<XOR1>(x)); x = fmaxf(x, dpp<XOR2>(x)); x = fmaxf(x, dpp<XOR7>(x)); x = fmaxf(x, dpp<ROR8>(x));
  auto s = __builtin_amdgcn_permlane16_swap(__float_as_uint(x), __float_as_uint(x), false, false);
  x = fmaxf(__uint_as_float(s[0]), __uint_as_float(s[1]));
  auto t = __builtin_amdgcn_permlane32_swap(__float_as_uint(x), __float_as_uint(x), false, false);
  return fmaxf(__uint_as_float(t[0]), __uint_as_float(t[1]));
}
__device__ __forceinline__ float wave_sum(float x) {
  x += dpp<XOR1>(x); x += dpp<XOR2>(x); x += dpp<XOR7>(x); x += dpp<ROR8>(x);
  x = swap16_add(x, x);
  return swap32_add(x, x);
}
template <int N> __device__ __forceinline__ void tr_reduce(float (&v)[N], int lane) {
#pragma unroll
  for (int i = 0; i < N / 2; ++i) v[i] = swap32_add(v[i], v[i + N / 2]);
#pragma unroll
  for (int i = 0; i < N / 4; ++i) v[i] = swap16_add(v[i], v[i + N / 4]);
  const bool b3 = (lane & 8) != 0;
#pragma unroll
  for (int i = 0; i < N / 8; ++i) {
    const float keep = b3 ? v[i + N / 8] : v[i], send = b3 ? v[i] : v[i + N / 8];
    v[i] = keep + dpp<ROR8>(send);
  }
}
#define BAR() asm volatile("s_waitcnt lgkmcnt(0)\n\ts_barrier" ::: "memory")
#define SCHED_FENCE() __builtin_amdgcn_sched_barrier(0)
__device__ __forceinline__ void lds_signal(unsigned* p, int lane) {
  asm volatile("s_waitcnt lgkmcnt(0)" ::: "memory");
  if (lane == 0) __hip_atomic_fetch_add(p, 1u, __ATOMIC_RELAXED, __HIP_MEMORY_SCOPE_WORKGROUP);
}
__device__ __forceinline__ void lds_wait(unsigned* p, unsigned n) {
  while (__builtin_amdgcn_readfirstlane(__hip_atomic_load(p, __ATOMIC_RELAXED, __HIP_MEMORY_SCOPE_WORKGROUP)) < n) __builtin_amdgcn_s_sleep(1);
  asm volatile("" ::: "memory");
}

template <int AUX> __device__ __forceinline__ void ld_tile(u32x4 (&t)[3], rsrc_t rs, int voff, int soff) {
  t[0] = __builtin_amdgcn_raw_buffer_load_b128(rs, voff, soff, AUX);
  t[1] = __builtin_amdgcn_raw_buffer_load_b128(rs, voff + 128, soff, AUX);
  t[2] = __builtin_amdgcn_raw_buffer_load_b128(rs, voff + 256, soff, AUX);
}
__device__ __forceinline__ void fma_tile(float (&acc)[24], const u32x4 (&t)[3], float p) {
#pragma unroll
  for (int s = 0; s < 3; ++s)
#pragma unroll
    for (int k = 0; k < 4; ++k) {
      acc[s * 8 + 2 * k] = fmaf(p, bflo(t[s][k]), acc[s * 8 + 2 * k]);
      acc[s * 8 + 2 * k + 1] = fmaf(p, bfhi(t[s][k]), acc[s * 8 + 2 * k + 1]);
    }
}
__device__ __forceinline__ void pin_acc(float (&a)[24]) {
  asm volatile("" : "+v"(a[0]), "+v"(a[1]), "+v"(a[2]), "+v"(a[3]), "+v"(a[4]), "+v"(a[5]), "+v"(a[6]), "+v"(a[7]),
               "+v"(a[8]), "+v"(a[9]), "+v"(a[10]), "+v"(a[11]), "+v"(a[12]), "+v"(a[13]), "+v"(a[14]), "+v"(a[15]),
               "+v"(a[16]), "+v"(a[17]), "+v"(a[18]), "+v"(a[19]), "+v"(a[20]), "+v"(a[21]), "+v"(a[22]), "+v"(a[23])
               :: "memory");
}
__device__ __forceinline__ void st_granule(rsrc_t rs, int off, unsigned tag, float v0, float v1) {
  const unsigned u0 = __float_as_uint(v0), u1 = __float_as_uint(v1);
  __builtin_amdgcn_raw_buffer_store_b128(u32x4{u0, u1, tag, (u0 ^ u1) + tag * 0x9e3779b1u}, rs, off, 0, AUX_SC1);
}
__device__ __forceinline__ bool granule_ok(const u32x4& g, unsigned tag) {
  return g[2] == tag && g[3] == (g[0] ^ g[1]) + tag * 0x9e3779b1u;
}

__device__ __forceinline__ void pace(unsigned& tnext) {
  if (DT == 0) return;
  tnext += DT;
  while ((int)((unsigned)__builtin_amdgcn_s_memrealtime() - tnext) < 0) __builtin_amdgcn_s_sleep(1);
}
struct __attribute__((aligned(16))) Smem {
  float pbuf[NSW * 256];
  float mrg[NSW * DH];
  float ml[NSW * 2];
  unsigned qpk[DH / 2];
  float hid[NB * 48];
  float gred[NAW * NB];
  unsigned tag[4];
  unsigned cnt[4];
  float act[KTOT * NB];
  union {
    u32x4 kl[NSW * LT * 3 * 64];
    unsigned short wsl[KTOT * NCOL];
  };
  float zred[30 * NB * NCOL];
  float fin[320];
};

#define STAMP(i) do {} while (0)

__global__ void __launch_bounds__(NTHR, 3)
fused_kernel(const unsigned short* __restrict__ x, const unsigned short* __restrict__ kc, const unsigned short* __restrict__ vc,
             const unsigned short* __restrict__ wq, const unsigned short* __restrict__ wo, const unsigned short* __restrict__ w1,
             const unsigned short* __restrict__ w2, const unsigned short* __restrict__ gw, unsigned short* __restrict__ out,
             unsigned char* __restrict__ ws) {
  __shared__ Smem sm;
  const int tid = threadIdx.x, lane = tid & 63, wave = __builtin_amdgcn_readfirstlane(tid >> 6);
  const int wg = blockIdx.x, xcd = wg & 7, idx = wg >> 3;
  const int h = xcd * 4 + (idx >> 3), j = idx & 7, b = j >> 1, sp = j & 1;
  const int r = lane >> 3, c = lane & 7;
  const rsrc_t rsW = mk_rsrc(ws, WS_BYTES);
  bool sec_aux = true; (void)sec_aux;
  STAMP(0);

  float acc[24];
#pragma unroll
  for (int i = 0; i < 24; ++i) acc[i] = 0.f;

  if (wave < NSW) {
    const rsrc_t rsK = mk_rsrc(kc, NB * NH * SK * ROWB), rsV = mk_rsrc(vc, NB * NH * SK * ROWB), rs0 = mk_rsrc(vc, 0);
    const int voff = r * ROWB + c * 16;
    const int base = ((b * NH + h) * SK + sp * KEYS_WG) * ROWB + wave * TILEB;
    constexpr int TS = NSW * TILEB;
    u32x4 buf[WIN][3];
    if (tid < 4) sm.cnt[tid] = 0u;
    BAR();
    unsigned tnext = (unsigned)__builtin_amdgcn_s_memrealtime() - DT + wave * (DT / NSW);
    {
      const unsigned lbase = (unsigned)(size_t)(__attribute__((address_space(3))) void*)&sm.kl[wave * LT * 3 * 64];
#pragma unroll
      for (int t = 0; t < LT; ++t) {
        pace(tnext);
#pragma unroll
        for (int s = 0; s < 3; ++s) {
          unsigned keep;
          asm volatile("s_mov_b32 %0, m0\n\ts_mov_b32 m0, %2\n\ts_nop 0\n\tglobal_load_lds_dwordx4 %1, %3 nt\n\ts_mov_b32 m0, %0"
                       : "=&s"(keep) : "v"((unsigned)(base + t * TS + voff + s * 128)), "s"(lbase + (t * 3 + s) * 1024), "s"(kc) : "memory");
        }
      }
    }
#pragma unroll
    for (int u = 0; u < WIN; ++u) {
      pace(tnext);
      ld_tile<AUX_NT>(buf[u], rsK, voff, base + (LT + u) * TS);
      SCHED_FENCE();
    }
    lds_wait(&sm.cnt[1], 2u);
    STAMP(1);
    u32x4 q[3];
#pragma unroll
    for (int s = 0; s < 3; ++s) q[s] = *reinterpret_cast<const u32x4*>(&sm.qpk[s * 32 + c * 4]);
    float* pw = &sm.pbuf[wave * 256 + r * 32];
    auto score = [&](const u32x4 (&t)[3]) -> float {
      float d0 = 0.f, d1 = 0.f, d2 = 0.f;
#pragma unroll
      for (int k = 0; k < 4; ++k) { d0 = dot2(t[0][k], q[0][k], d0); d1 = dot2(t[1][k], q[1][k], d1); d2 = dot2(t[2][k], q[2][k], d2); }
      return (d0 + d1) + d2;
    };
    auto rowsum = [&](float d) -> float { d += dpp<XOR1>(d); d += dpp<XOR2>(d); d += dpp<XOR7>(d); return d; };
    float sc = 0.f;
    asm volatile("s_waitcnt vmcnt(%0)" :: "n"(3 * WIN) : "memory");
#pragma unroll
    for (int t = 0; t < LT; ++t) {
      u32x4 kt[3];
#pragma unroll
      for (int s = 0; s < 3; ++s) kt[s] = sm.kl[((wave * LT + t) * 3 + s) * 64 + lane];
      const float d = rowsum(score(kt));
      sc = (c == t) ? d : sc;
    }
    lds_signal(&sm.cnt[2], lane);
#pragma unroll 1
    for (int it = 0; it < 3; ++it) {
      const rsrc_t rn = it < 2 ? rsK : rsV;
      const int na = base + (LT + 8 + it * 8) * TS, nb = it < 2 ? na : base - LT * TS;
#pragma unroll
      for (int u = 0; u < 8; ++u) {
        const float d0 = score(buf[u]);
        pace(tnext);
        SCHED_FENCE();
        if (u < 4) ld_tile<AUX_NT>(buf[u], rsK, voff, na + u * TS);
        else ld_tile<AUX_NT>(buf[u], rn, voff, nb + u * TS);
        SCHED_FENCE();
        const float d = rowsum(d0);
        sc = (c == ((u + LT) & 7)) ? d : sc;
        if (u == 3) pw[it * 8 + c] = sc;
      }
    }
#pragma unroll
    for (int u = 0; u < 4; ++u) {
      const float d0 = score(buf[u]);
      pace(tnext);
      SCHED_FENCE();
      ld_tile<AUX_NT>(buf[u], rsV, voff, base + (4 + u) * TS);
      SCHED_FENCE();
      const float d = rowsum(d0);
      sc = (c == ((u + LT) & 7)) ? d : sc;
    }
    pw[3 * 8 + c] = sc;
    STAMP(2);
    constexpr float C2 = 0.07216878364870322f * 1.4426950408889634f;
    float s4[4], m = -INFINITY;
#pragma unroll
    for (int it = 0; it < 4; ++it) { s4[it] = pw[it * 8 + c] * C2; m = fmaxf(m, s4[it]); }
    m = wave_max(m);
    float l = 0.f;
#pragma unroll
    for (int it = 0; it < 4; ++it) { const float p = __builtin_amdgcn_exp2f(s4[it] - m); l += p; pw[it * 8 + c] = p; }
    l = wave_sum(l);
#pragma unroll 1
    for (int it = 0; it < 4; ++it) {
      const rsrc_t rn = it < 3 ? rsV : rs0;
      const int nb = base + (it + 1) * 8 * TS;
      const f32x4 pa = *reinterpret_cast<const f32x4*>(pw + it * 8), pb = *reinterpret_cast<const f32x4*>(pw + it * 8 + 4);
#pragma unroll
      for (int u = 0; u < 8; ++u) {
        fma_tile(acc, buf[(u + LT) & 7], u < 4 ? pa[u & 3] : pb[u & 3]);
        pin_acc(acc);
        if (it < 3) pace(tnext);
        SCHED_FENCE();
        ld_tile<AUX_NT>(buf[(u + LT) & 7], rn, voff, nb + u * TS);
        SCHED_FENCE();
      }
    }
    STAMP(3);
    tr_reduce<24>(acc, lane);
    const int a0 = 3 * ((lane >> 3) & 1) + 6 * ((lane >> 4) & 1) + 12 * (lane >> 5);
#pragma unroll
    for (int i = 0; i < 3; ++i) { const int a = a0 + i; sm.mrg[wave * DH + (a >> 3) * 64 + c * 8 + (a & 7)] = acc[i]; }
    if (lane == 0) { sm.ml[wave * 2] = m; sm.ml[wave * 2 + 1] = l; }
  } else {
    __builtin_amdgcn_s_setprio(3);
    const int v = wave - NSW, row0 = v * 160, at = tid - NSW * 64;
    const unsigned tag = __hip_atomic_load((GAS unsigned*)(ws + WS_EPOCH), RLX_AGENT) + 1u;
    {
      const rsrc_t rsX = mk_rsrc(x, NB * DM * 2);
      const int g = lane >= 30 ? 1 : 0, ch = lane < 60 ? lane - g * 30 : 0;
      const int qrow = j * 80 + v * 20 + g * 10;
      const bool isq = ch < 24;
      const char* src = isq ? (const char*)wq + (size_t)qrow * (NH * DH * 2) + h * ROWB + ch * 16
                            : (const char*)w1 + (size_t)qrow * (FF * 2) + h * 96 + (ch - 24) * 16;
      const int rstride = isq ? NH * DH * 2 : FF * 2;
      u32x4 xa[4]; unsigned xb[4];
#pragma unroll
      for (int bq = 0; bq < 4; ++bq) {
        xa[bq] = __builtin_amdgcn_raw_buffer_load_b128(rsX, qrow * 2, bq * DM * 2, 0);
        xb[bq] = __builtin_amdgcn_raw_buffer_load_b32(rsX, qrow * 2 + 16, bq * DM * 2, 0);
      }
      u32x4 wr[10];
#pragma unroll
      for (int i = 0; i < 10; ++i) wr[i] = __builtin_nontemporal_load(reinterpret_cast<const u32x4*>(src + i * rstride));
      SCHED_FENCE();
      BAR();
      float qa[4][8];
#pragma unroll
      for (int bq = 0; bq < 4; ++bq)
#pragma unroll
        for (int k = 0; k < 8; ++k) qa[bq][k] = 0.f;
#pragma unroll
      for (int p = 0; p < 5; ++p)
#pragma unroll
        for (int k = 0; k < 4; ++k) {
          const unsigned lo = __builtin_amdgcn_perm(wr[2 * p + 1][k], wr[2 * p][k], 0x05040100u);
          const unsigned hi = __builtin_amdgcn_perm(wr[2 * p + 1][k], wr[2 * p][k], 0x07060302u);
#pragma unroll
          for (int bq = 0; bq < 4; ++bq) {
            const unsigned xp = p < 4 ? xa[bq][p & 3] : xb[bq];
            qa[bq][2 * k] = dot2(lo, xp, qa[bq][2 * k]);
            qa[bq][2 * k + 1] = dot2(hi, xp, qa[bq][2 * k + 1]);
          }
        }
      if (lane < 60) {
        float* dst = &sm.zred[(v * 2 + g) * (NB * 2 * QP) + ch * 8];
#pragma unroll
        for (int bq = 0; bq < 4; ++bq) {
          *reinterpret_cast<f32x4*>(dst + bq * 2 * QP) = f32x4{qa[bq][0], qa[bq][1], qa[bq][2], qa[bq][3]};
          *reinterpret_cast<f32x4*>(dst + bq * 2 * QP + 4) = f32x4{qa[bq][4], qa[bq][5], qa[bq][6], qa[bq][7]};
        }
      }
    }
    lds_signal(&sm.cnt[0], lane);
    lds_wait(&sm.cnt[0], 4u);
    STAMP(6);
#pragma unroll
    for (int i = 0; i < 2; ++i) {
      const int e = i * 256 + at;
      if (e < NB * QP) {
        const int bq = e / QP, dp = e - bq * QP;
        float q0 = 0.f, q1 = 0.f;
#pragma unroll
        for (int w = 0; w < 8; ++w) { const f32x2 t = *reinterpret_cast<const f32x2*>(&sm.zred[w * (NB * 2 * QP) + e * 2]); q0 += t[0]; q1 += t[1]; }
        const unsigned u0 = __float_as_uint(q0), u1 = __float_as_uint(q1);
        __builtin_amdgcn_raw_buffer_store_b128(u32x4{u0, u1, tag, (u0 ^ u1) + tag * 0x9e3779b1u}, rsW, WS_Q + (((h * NB + bq) * 8 + j) * QP + dp) * 16, 0, AUX_SC1);
      }
    }
    {
      const int t = at & 127;
      const bool act = t < 96;
      const int gb = v < 2 ? b : (act ? t / 24 : 0), gp = v < 2 ? (act ? t : 0) : 96 + (act ? t % 24 : 0);
      const int go = WS_Q + ((h * NB + gb) * 8 * QP + gp) * 16;
      float q0, q1;
      bool okq = true;
      __builtin_amdgcn_s_sleep(POLLDELAY);
      for (unsigned spin = 0;; ++spin) {
        asm volatile("" ::: "memory");
        u32x4 g[8];
#pragma unroll
        for (int w = 0; w < 8; ++w) g[w] = __builtin_amdgcn_raw_buffer_load_b128(rsW, go + w * QP * 16, 0, AUX_SC1);
        bool ok = true;
        q0 = 0.f; q1 = 0.f;
#pragma unroll
        for (int w = 0; w < 8; ++w) {
          ok = ok && g[w][2] == tag && g[w][3] == (g[w][0] ^ g[w][1]) + tag * 0x9e3779b1u;
          q0 += __uint_as_float(g[w][0]); q1 += __uint_as_float(g[w][1]);
        }
        if (__all(ok)) break;
        if (spin > SPIN_CAP) { okq = false; break; }
      }
      if (v < 2) {
        if (act) sm.qpk[t] = okq ? cvtpk(q0, q1) : 0x7fc07fc0u;
        lds_signal(&sm.cnt[1], lane);
      } else if (act) {
        const float h0 = rbf(q0), h1 = rbf(q1);
        const float s0 = rbf(h0 / (1.f + __expf(-h0))), s1 = rbf(h1 / (1.f + __expf(-h1)));
        *reinterpret_cast<f32x2*>(&sm.hid[t * 2]) = okq ? f32x2{s0, s1} : f32x2{__builtin_nanf(""), __builtin_nanf("")};
      }
    }
    STAMP(1);
    {
      const unsigned* x32 = reinterpret_cast<const unsigned*>(x);
      const unsigned* g32 = reinterpret_cast<const unsigned*>(gw);
      unsigned xv[5], gv[5];
#pragma unroll
      for (int i = 0; i < 5; ++i) { const int e = i * 64 + lane, bb = e / 80, dd = e - bb * 80; xv[i] = x32[bb * (DM / 2) + row0 / 2 + dd]; gv[i] = g32[row0 / 2 + dd]; }
      u32x4 wv[10];
      const rsrc_t rsO = mk_rsrc(wo, NH * DH * DM * 2), rs2 = mk_rsrc(w2, FF * DM * 2);
#pragma unroll
      for (int i = 0; i < 10; ++i) {
        const int e = i * 256 + at, row = e / 10, ch = e - row * 10;
        const bool isO = i < 7 || (i == 7 && v < 2);
        const int off = e < KTOT * 10 ? ((isO ? h * DH + row : h * 48 + row - DH) * DM + j * NCOL + ch * 8) * 2 : 0x7ffffff0;
        wv[i] = __builtin_amdgcn_raw_buffer_load_b128(isO ? rsO : rs2, off, 0, AUX_NT);
      }
      SCHED_FENCE();
      if (at == 0) sm.tag[0] = tag;
      float gp[4] = {0.f, 0.f, 0.f, 0.f};
#pragma unroll
      for (int i = 0; i < 5; ++i) {
        const int e = i * 64 + lane, bb = e / 80;
        const float pr = rbf(bflo(xv[i]) * bflo(gv[i])) + rbf(bfhi(xv[i]) * bfhi(gv[i]));
#pragma unroll
        for (int bq = 0; bq < 4; ++bq) gp[bq] += (bb == bq) ? pr : 0.f;
      }
#pragma unroll
      for (int bq = 0; bq < 4; ++bq) { const float s = wave_sum(gp[bq]); if (lane == 0) sm.gred[v * 4 + bq] = s; }
      STAMP(3);
      lds_wait(&sm.cnt[2], (unsigned)NSW);
#pragma unroll
      for (int i = 0; i < 10; ++i) { const int e = i * 256 + at; if (e < KTOT * 10) *reinterpret_cast<u32x4*>(&sm.wsl[e * 8]) = wv[i]; }
      STAMP(5);
    }
    STAMP(2);
  }
  BAR();
  sec_aux = false;
  STAMP(4);
  const unsigned tag = sm.tag[0];
  if (tid < 194) {
    float M = sm.ml[0];
#pragma unroll
    for (int w = 1; w < NSW; ++w) M = fmaxf(M, sm.ml[2 * w]);
    float o = 0.f;
#pragma unroll
    for (int w = 0; w < NSW; ++w) {
      const float e = __builtin_amdgcn_exp2f(sm.ml[2 * w] - M);
      o = fmaf(e, tid < DH ? sm.mrg[w * DH + tid] : sm.ml[2 * w + 1], o);
    }
    const float on = __shfl_down(o, 1);
    if (!(tid & 1)) st_granule(rsW, WS_S1 + wg * S1_STRIDE + tid * 8, tag, tid == 192 ? M : o, on);
  }
  float gate;
  {
    const int bb = tid < 768 ? tid / DH : 0;
    float gs = (sm.gred[bb] + sm.gred[4 + bb]) + (sm.gred[8 + bb] + sm.gred[12 + bb]);
    gs = rbf(gs);
    gate = rbf(1.f / (1.f + __expf(-gs)));
    const int d = tid - bb * DH;
    if (d < 48) sm.act[(DH + d) * NB + bb] = sm.hid[bb * 48 + d] * rbf(1.f - gate);
  }
  bool okall = true;
  {
    const int bb = tid / DH, d = tid - bb * DH;
    const int p0 = xcd + 8 * ((h & 3) * 8 + bb * 2), p1 = p0 + 8;
    const int o0 = WS_S1 + p0 * S1_STRIDE, o1 = WS_S1 + p1 * S1_STRIDE;
    u32x4 g0, g1, m0, m1;
    for (unsigned spin = 0;; ++spin) {
      asm volatile("" ::: "memory");
      g0 = __builtin_amdgcn_raw_buffer_load_b128(rsW, o0 + (d >> 1) * 16, 0, AUX_SC1);
      g1 = __builtin_amdgcn_raw_buffer_load_b128(rsW, o1 + (d >> 1) * 16, 0, AUX_SC1);
      m0 = __builtin_amdgcn_raw_buffer_load_b128(rsW, o0 + 192 * 8, 0, AUX_SC1);
      m1 = __builtin_amdgcn_raw_buffer_load_b128(rsW, o1 + 192 * 8, 0, AUX_SC1);
      const bool ok = granule_ok(g0, tag) && granule_ok(g1, tag) && granule_ok(m0, tag) && granule_ok(m1, tag);
      if (__all(ok)) break;
      if (spin > SPIN_CAP) { okall = false; break; }
      __builtin_amdgcn_s_sleep(1);
    }
    const float M0 = __uint_as_float(m0[0]), L0 = __uint_as_float(m0[1]), M1 = __uint_as_float(m1[0]), L1 = __uint_as_float(m1[1]);
    const float M = fmaxf(M0, M1), e0 = __builtin_amdgcn_exp2f(M0 - M), e1 = __builtin_amdgcn_exp2f(M1 - M);
    const float o0v = __uint_as_float((d & 1) ? g0[1] : g0[0]), o1v = __uint_as_float((d & 1) ? g1[1] : g1[0]);
    const float o = (e0 * o0v + e1 * o1v) / (e0 * L0 + e1 * L1);
    sm.act[d * NB + bb] = okall ? rbf(o) * gate : __builtin_nanf("");
  }
  STAMP(5);
  BAR();
  if (tid < 600) {
    const int kg = tid / 20, cq = tid - kg * 20;
    float z[4][4];
#pragma unroll
    for (int i = 0; i < 4; ++i)
#pragma unroll
      for (int k = 0; k < 4; ++k) z[i][k] = 0.f;
#pragma unroll
    for (int rr = 0; rr < 8; ++rr) {
      const int row = kg * 8 + rr;
      const u32x2 wv = *reinterpret_cast<const u32x2*>(&sm.wsl[row * NCOL + cq * 4]);
      const f32x4 a = *reinterpret_cast<const f32x4*>(&sm.act[row * NB]);
      const float w0 = bflo(wv[0]), w1f = bfhi(wv[0]), w2f = bflo(wv[1]), w3 = bfhi(wv[1]);
#pragma unroll
      for (int i = 0; i < 4; ++i) { z[i][0] = fmaf(a[i], w0, z[i][0]); z[i][1] = fmaf(a[i], w1f, z[i][1]); z[i][2] = fmaf(a[i], w2f, z[i][2]); z[i][3] = fmaf(a[i], w3, z[i][3]); }
    }
#pragma unroll
    for (int i = 0; i < 4; ++i) *reinterpret_cast<f32x4*>(&sm.zred[kg * 320 + i * NCOL + cq * 4]) = f32x4{z[i][0], z[i][1], z[i][2], z[i][3]};
  }
  BAR();
  if (tid < 320) {
    float z = 0.f;
#pragma unroll
    for (int kg = 0; kg < 30; ++kg) z += sm.zred[kg * 320 + tid];
    const int bb = tid / 80, col = tid - bb * 80, f = (j * 4 + bb) * 8 + col / 10;
    const float zn = __shfl_down(z, 1);
    if (!(tid & 1)) st_granule(rsW, WS_S2 + f * S2_STRIDE + (h * 10 + col % 10) * 8, tag, z, zn);
    u32x4 g;
    bool ok2 = true;
    for (unsigned spin = 0;; ++spin) {
      asm volatile("" ::: "memory");
      g = __builtin_amdgcn_raw_buffer_load_b128(rsW, WS_S2 + wg * S2_STRIDE + (tid >> 1) * 16, 0, AUX_SC1);
      if (__all(granule_ok(g, tag))) break;
      if (spin > SPIN_CAP) { ok2 = false; break; }
      __builtin_amdgcn_s_sleep(1);
    }
    sm.fin[tid] = ok2 ? __uint_as_float((tid & 1) ? g[1] : g[0]) : __builtin_nanf("");
  }
  STAMP(6);
  BAR();
  if (tid < 10) {
    float z = 0.f;
#pragma unroll
    for (int hh = 0; hh < NH; ++hh) z += sm.fin[hh * 10 + tid];
    const int jj = wg >> 5, bb = (wg >> 3) & 3, sub = wg & 7;
    out[bb * DM + jj * NCOL + sub * 10 + tid] = (unsigned short)(cvtpk(z, z) & 0xffffu);
    if (wg == 0 && tid == 0) __hip_atomic_store((GAS unsigned*)(ws + WS_EPOCH), tag, RLX_AGENT);
  }
  STAMP(7);
}

}

extern "C" void kernel_launch(void* const* d_in, const int* in_sizes, int n_in, void* d_out, int out_size, void* d_ws,
                              size_t ws_size, hipStream_t stream) {
  (void)in_sizes; (void)n_in; (void)out_size; (void)ws_size;
  const unsigned short* ax = (const unsigned short*)d_in[0];
  const unsigned short* kc = (const unsigned short*)d_in[1];
  const unsigned short* vc = (const unsigned short*)d_in[2];
  const unsigned short* wq = (const unsigned short*)d_in[3];
  const unsigned short* wo = (const unsigned short*)d_in[4];
  const unsigned short* w1 = (const unsigned short*)d_in[6];
  const unsigned short* w2 = (const unsigned short*)d_in[7];
  const unsigned short* gw = (const unsigned short*)d_in[8];
  fused_kernel<<<NWG, NTHR, 0, stream>>>(ax, kc, vc, wq, wo, w1, w2, gw, (unsigned short*)d_out, (unsigned char*)d_ws);
}
```

```cpp
#include <hip/hip_runtime.h>
#include <hip/hip_bf16.h>
#include <cstdint>

namespace {

constexpr int NB = 4, DM = 640, NH = 32, SK = 4096, DH = 192, FF = 1536;
constexpr int NWG = 256, NSW = 8, NAW = 4, NTHR = (NSW + NAW) * 64;
constexpr int ROWB = DH * 2;
constexpr int TILEB = 8 * ROWB;
constexpr int KEYS_WG = SK / 2;
constexpr int KEYS_W = KEYS_WG / NSW;
#define POLLDELAY 60
#define DT_ 0
constexpr unsigned DT = DT_;
constexpr int LT = 4;
constexpr int WIN = 8;
constexpr int KTOT = DH + 48;
constexpr int NCOL = 80;

constexpr int WS_EPOCH = 0;
constexpr int WS_S1 = 4096;
constexpr int S1_STRIDE = 256 * 8;
constexpr int WS_S2 = WS_S1 + NWG * S1_STRIDE;
constexpr int S2_STRIDE = 320 * 8;
constexpr int QP = (DH + 48) / 2;
constexpr int WS_Q = WS_S2 + NWG * S2_STRIDE;
constexpr int WS_STAMP = WS_Q + NH * NB * 8 * QP * 16;
constexpr int WS_BYTES = WS_STAMP + NWG * 16 * 8;

typedef unsigned u32x4 __attribute__((ext_vector_type(4)));
typedef unsigned u32x2 __attribute__((ext_vector_type(2)));
typedef float f32x4 __attribute__((ext_vector_type(4)));
typedef float f32x2 __attribute__((ext_vector_type(2)));
typedef __bf16 bf16x2 __attribute__((ext_vector_type(2)));
typedef __amdgpu_buffer_rsrc_t rsrc_t;
#define GAS __attribute__((address_space(1)))
#define RLX_AGENT __ATOMIC_RELAXED, __HIP_MEMORY_SCOPE_AGENT
constexpr int AUX_NT = 2, AUX_SC1 = 16;
constexpr unsigned SPIN_CAP = 1u << 18;

__device__ __forceinline__ rsrc_t mk_rsrc(const void* p, unsigned bytes) {
  return __builtin_amdgcn_make_buffer_rsrc(const_cast<void*>(p), (short)0, (int)bytes, 0x00020000);
}
__device__ __forceinline__ float bflo(unsigned u) { return __builtin_bit_cast(float, u << 16); }
__device__ __forceinline__ float bfhi(unsigned u) { return __builtin_bit_cast(float, u & 0xffff0000u); }
__device__ __forceinline__ unsigned cvtpk(float lo, float hi) {
  unsigned r; asm("v_cvt_pk_bf16_f32 %0, %1, %2" : "=v"(r) : "v"(lo), "v"(hi)); return r;
}
__device__ __forceinline__ float rbf(float x) { return bflo(cvtpk(x, x)); }
__device__ __forceinline__ float dot2(unsigned k, unsigned q, float acc) {
  return __builtin_amdgcn_fdot2_f32_bf16(__builtin_bit_cast(bf16x2, k), __builtin_bit_cast(bf16x2, q), acc, false);
}
template <int CTRL> __device__ __forceinline__ float dpp(float x) {
  return __builtin_bit_cast(float, __builtin_amdgcn_mov_dpp(__builtin_bit_cast(int, x), CTRL, 0xf, 0xf, true));
}
constexpr int XOR1 = 0xB1, XOR2 = 0x4E, XOR7 = 0x141, ROR8 = 0x128;
__device__ __forceinline__ float swap32_add(float a, float b) {
  auto s = __builtin_amdgcn_permlane32_swap(__float_as_uint(a), __float_as_uint(b), false, false);
  return __uint_as_float(s[0]) + __uint_as_float(s[1]);
}
__device__ __forceinline__ float swap16_add(float a, float b) {
  auto s = __builtin_amdgcn_permlane16_swap(__float_as_uint(a), __float_as_uint(b), false, false);
  return __uint_as_float(s[0]) + __uint_as_float(s[1]);
}
__device__ __forceinline__ float wave_max(float x) {
  x = fmaxf(x, dpp<XOR1>(x)); x = fmaxf(x, dpp<XOR2>(x)); x = fmaxf(x, dpp<XOR7>(x)); x = fmaxf(x, dpp<ROR8>(x));
  auto s = __builtin_amdgcn_permlane16_swap(__float_as_uint(x), __float_as_uint(x), false, false);
  x = fmaxf(__uint_as_float(s[0]), __uint_as_float(s[1]));
  auto t = __builtin_amdgcn_permlane32_swap(__float_as_uint(x), __float_as_uint(x), false, false);
  return fmaxf(__uint_as_float(t[0]), __uint_as_float(t[1]));
}
__device__ __forceinline__ float wave_sum(float x) {
  x += dpp<XOR1>(x); x += dpp<XOR2>(x); x += dpp<XOR7>(x); x += dpp<ROR8>(x);
  x = swap16_add(x, x);
  return swap32_add(x, x);
}
template <int N> __device__ __forceinline__ void tr_reduce(float (&v)[N], int lane) {
#pragma unroll
  for (int i = 0; i < N / 2; ++i) v[i] = swap32_add(v[i], v[i + N / 2]);
#pragma unroll
  for (int i = 0; i < N / 4; ++i) v[i] = swap16_add(v[i], v[i + N / 4]);
  const bool b3 = (lane & 8) != 0;
#pragma unroll
  for (int i = 0; i < N / 8; ++i) {
    const float keep = b3 ? v[i + N / 8] : v[i], send = b3 ? v[i] : v[i + N / 8];
    v[i] = keep + dpp<ROR8>(send);
  }
}
#define BAR() asm volatile("s_waitcnt lgkmcnt(0)\n\ts_barrier" ::: "memory")
#define SCHED_FENCE() __builtin_amdgcn_sched_barrier(0)
__device__ __forceinline__ void lds_signal(unsigned* p, int lane) {
  asm volatile("s_waitcnt lgkmcnt(0)" ::: "memory");
  if (lane == 0) __hip_atomic_fetch_add(p, 1u, __ATOMIC_RELAXED, __HIP_MEMORY_SCOPE_WORKGROUP);
}
__device__ __forceinline__ void lds_wait(unsigned* p, unsigned n) {
  while (__builtin_amdgcn_readfirstlane(__hip_atomic_load(p, __ATOMIC_RELAXED, __HIP_MEMORY_SCOPE_WORKGROUP)) < n) __builtin_amdgcn_s_sleep(1);
  asm volatile("" ::: "memory");
}

template <int AUX> __device__ __forceinline__ void ld_tile(u32x4 (&t)[3], rsrc_t rs, int voff, int soff) {
  t[0] = __builtin_amdgcn_raw_buffer_load_b128(rs, voff, soff, AUX);
  t[1] = __builtin_amdgcn_raw_buffer_load_b128(rs, voff + 128, soff, AUX);
  t[2] = __builtin_amdgcn_raw_buffer_load_b128(rs, voff + 256, soff, AUX);
}
__device__ __forceinline__ void fma_tile(float (&acc)[24], const u32x4 (&t)[3], float p) {
#pragma unroll
  for (int s = 0; s < 3; ++s)
#pragma unroll
    for (int k = 0; k < 4; ++k) {
      acc[s * 8 + 2 * k] = fmaf(p, bflo(t[s][k]), acc[s * 8 + 2 * k]);
      acc[s * 8 + 2 * k + 1] = fmaf(p, bfhi(t[s][k]), acc[s * 8 + 2 * k + 1]);
    }
}
__device__ __forceinline__ void pin_acc(float (&a)[24]) {
  asm volatile("" : "+v"(a[0]), "+v"(a[1]), "+v"(a[2]), "+v"(a[3]), "+v"(a[4]), "+v"(a[5]), "+v"(a[6]), "+v"(a[7]),
               "+v"(a[8]), "+v"(a[9]), "+v"(a[10]), "+v"(a[11]), "+v"(a[12]), "+v"(a[13]), "+v"(a[14]), "+v"(a[15]),
               "+v"(a[16]), "+v"(a[17]), "+v"(a[18]), "+v"(a[19]), "+v"(a[20]), "+v"(a[21]), "+v"(a[22]), "+v"(a[23])
               :: "memory");
}
__device__ __forceinline__ void st_granule(rsrc_t rs, int off, unsigned tag, float v0, float v1) {
  const unsigned u0 = __float_as_uint(v0), u1 = __float_as_uint(v1);
  __builtin_amdgcn_raw_buffer_store_b128(u32x4{u0, u1, tag, (u0 ^ u1) + tag * 0x9e3779b1u}, rs, off, 0, AUX_SC1);
}
__device__ __forceinline__ bool granule_ok(const u32x4& g, unsigned tag) {
  return g[2] == tag && g[3] == (g[0] ^ g[1]) + tag * 0x9e3779b1u;
}

__device__ __forceinline__ void pace(unsigned& tnext) {
  if (DT == 0) return;
  tnext += DT;
  while ((int)((unsigned)__builtin_amdgcn_s_memrealtime() - tnext) < 0) __builtin_amdgcn_s_sleep(1);
}
struct __attribute__((aligned(16))) Smem {
  float pbuf[NSW * 256];
  float mrg[NSW * DH];
  float ml[NSW * 2];
  unsigned qpk[DH / 2];
  float hid[NB * 48];
  float gred[NAW * NB];
  unsigned tag[4];
  unsigned cnt[4];
  float act[KTOT * NB];
  union {
    u32x4 kl[NSW * LT * 3 * 64];
    unsigned short wsl[KTOT * NCOL];
  };
  float zred[30 * NB * NCOL];
  float fin[320];
};

#define STAMP(i) do {} while (0)

__global__ void __launch_bounds__(NTHR, 3)
fused_kernel(const unsigned short* __restrict__ x, const unsigned short* __restrict__ kc, const unsigned short* __restrict__ vc,
             const unsigned short* __restrict__ wq, const unsigned short* __restrict__ wo, const unsigned short* __restrict__ w1,
             const unsigned short* __restrict__ w2, const unsigned short* __restrict__ gw, unsigned short* __restrict__ out,
             unsigned char* __restrict__ ws) {
  __shared__ Smem sm;
  unsigned long long code_pc; int code_len;
  asm volatile("s_getpc_b64 %0\n.Lkcode_a:\n\ts_mov_b32 %1, .Lkcode_b-.Lkcode_a" : "=s"(code_pc), "=s"(code_len));
  const int tid = threadIdx.x, lane = tid & 63, wave = __builtin_amdgcn_readfirstlane(tid >> 6);
  const int wg = blockIdx.x, xcd = wg & 7, idx = wg >> 3;
  const int h = xcd * 4 + (idx >> 3), j = idx & 7, b = j >> 1, sp = j & 1;
  const int r = lane >> 3, c = lane & 7;
  const rsrc_t rsW = mk_rsrc(ws, WS_BYTES);
  bool sec_aux = true; (void)sec_aux;
  STAMP(0);

  float acc[24];
#pragma unroll
  for (int i = 0; i < 24; ++i) acc[i] = 0.f;

  if (wave < NSW) {
    const rsrc_t rsK = mk_rsrc(kc, NB * NH * SK * ROWB), rsV = mk_rsrc(vc, NB * NH * SK * ROWB), rs0 = mk_rsrc(vc, 0);
    const int voff = r * ROWB + c * 16;
    const int base = ((b * NH + h) * SK + sp * KEYS_WG) * ROWB + wave * TILEB;
    constexpr int TS = NSW * TILEB;
    u32x4 buf[WIN][3];
    unsigned pf;
    {
      const unsigned off = min((unsigned)(wave * 64 + lane) * 128u, (unsigned)max(code_len - 4, 0)) & ~3u;
      asm volatile("global_load_dword %0, %1, %2" : "=v"(pf) : "v"(off), "s"(code_pc) : "memory");
    }
    if (tid < 4) sm.cnt[tid] = 0u;
    BAR();
    unsigned tnext = (unsigned)__builtin_amdgcn_s_memrealtime() - DT + wave * (DT / NSW);
    {
      const unsigned lbase = (unsigned)(size_t)(__attribute__((address_space(3))) void*)&sm.kl[wave * LT * 3 * 64];
#pragma unroll
      for (int t = 0; t < LT; ++t) {
        pace(tnext);
#pragma unroll
        for (int s = 0; s < 3; ++s) {
          unsigned keep;
          asm volatile("s_mov_b32 %0, m0\n\ts_mov_b32 m0, %2\n\ts_nop 0\n\tglobal_load_lds_dwordx4 %1, %3 nt\n\ts_mov_b32 m0, %0"
                       : "=&s"(keep) : "v"((unsigned)(base + t * TS + voff + s * 128)), "s"(lbase + (t * 3 + s) * 1024), "s"(kc) : "memory");
        }
      }
    }
#pragma unroll
    for (int u = 0; u < WIN; ++u) {
      pace(tnext);
      ld_tile<AUX_NT>(buf[u], rsK, voff, base + (LT + u) * TS);
      SCHED_FENCE();
    }
    lds_wait(&sm.cnt[1], 2u);
    STAMP(1);
    u32x4 q[3];
#pragma unroll
    for (int s = 0; s < 3; ++s) q[s] = *reinterpret_cast<const u32x4*>(&sm.qpk[s * 32 + c * 4]);
    float* pw = &sm.pbuf[wave * 256 + r * 32];
    auto score = [&](const u32x4 (&t)[3]) -> float {
      float d0 = 0.f, d1 = 0.f, d2 = 0.f;
#pragma unroll
      for (int k = 0; k < 4; ++k) { d0 = dot2(t[0][k], q[0][k], d0); d1 = dot2(t[1][k], q[1][k], d1); d2 = dot2(t[2][k], q[2][k], d2); }
      return (d0 + d1) + d2;
    };
    auto rowsum = [&](float d) -> float { d += dpp<XOR1>(d); d += dpp<XOR2>(d); d += dpp<XOR7>(d); return d; };
    float sc = 0.f;
    asm volatile("s_waitcnt vmcnt(%0)" :: "n"(3 * WIN) : "memory");
    asm volatile("" :: "v"(pf));
#pragma unroll
    for (int t = 0; t < LT; ++t) {
      u32x4 kt[3];
#pragma unroll
      for (int s = 0; s < 3; ++s) kt[s] = sm.kl[((wave * LT + t) * 3 + s) * 64 + lane];
      const float d = rowsum(score(kt));
      sc = (c == t) ? d : sc;
    }
    lds_signal(&sm.cnt[2], lane);
#pragma unroll 1
    for (int it = 0; it < 3; ++it) {
      const rsrc_t rn = it < 2 ? rsK : rsV;
      const int na = base + (LT + 8 + it * 8) * TS, nb = it < 2 ? na : base - LT * TS;
#pragma unroll
      for (int u = 0; u < 8; ++u) {
        const float d0 = score(buf[u]);
        pace(tnext);
        SCHED_FENCE();
        if (u < 4) ld_tile<AUX_NT>(buf[u], rsK, voff, na + u * TS);
        else ld_tile<AUX_NT>(buf[u], rn, voff, nb + u * TS);
        SCHED_FENCE();
        const float d = rowsum(d0);
        sc = (c == ((u + LT) & 7)) ? d : sc;
        if (u == 3) pw[it * 8 + c] = sc;
      }
    }
#pragma unroll
    for (int u = 0; u < 4; ++u) {
      const float d0 = score(buf[u]);
      pace(tnext);
      SCHED_FENCE();
      ld_tile<AUX_NT>(buf[u], rsV, voff, base + (4 + u) * TS);
      SCHED_FENCE();
      const float d = rowsum(d0);
      sc = (c == ((u + LT) & 7)) ? d : sc;
    }
    pw[3 * 8 + c] = sc;
    STAMP(2);
    constexpr float C2 = 0.07216878364870322f * 1.4426950408889634f;
    float s4[4], m = -INFINITY;
#pragma unroll
    for (int it = 0; it < 4; ++it) { s4[it] = pw[it * 8 + c] * C2; m = fmaxf(m, s4[it]); }
    m = wave_max(m);
    float l = 0.f;
#pragma unroll
    for (int it = 0; it < 4; ++it) { const float p = __builtin_amdgcn_exp2f(s4[it] - m); l += p; pw[it * 8 + c] = p; }
    l = wave_sum(l);
#pragma unroll 1
    for (int it = 0; it < 4; ++it) {
      const rsrc_t rn = it < 3 ? rsV : rs0;
      const int nb = base + (it + 1) * 8 * TS;
      const f32x4 pa = *reinterpret_cast<const f32x4*>(pw + it * 8), pb = *reinterpret_cast<const f32x4*>(pw + it * 8 + 4);
#pragma unroll
      for (int u = 0; u < 8; ++u) {
        fma_tile(acc, buf[(u + LT) & 7], u < 4 ? pa[u & 3] : pb[u & 3]);
        pin_acc(acc);
        if (it < 3) pace(tnext);
        SCHED_FENCE();
        ld_tile<AUX_NT>(buf[(u + LT) & 7], rn, voff, nb + u * TS);
        SCHED_FENCE();
      }
    }
    STAMP(3);
    tr_reduce<24>(acc, lane);
    const int a0 = 3 * ((lane >> 3) & 1) + 6 * ((lane >> 4) & 1) + 12 * (lane >> 5);
#pragma unroll
    for (int i = 0; i < 3; ++i) { const int a = a0 + i; sm.mrg[wave * DH + (a >> 3) * 64 + c * 8 + (a & 7)] = acc[i]; }
    if (lane == 0) { sm.ml[wave * 2] = m; sm.ml[wave * 2 + 1] = l; }
  } else {
    __builtin_amdgcn_s_setprio(3);
    const int v = wave - NSW, row0 = v * 160, at = tid - NSW * 64;
    const unsigned tag = __hip_atomic_load((GAS unsigned*)(ws + WS_EPOCH), RLX_AGENT) + 1u;
    {
      const rsrc_t rsX = mk_rsrc(x, NB * DM * 2);
      const int g = lane >= 30 ? 1 : 0, ch = lane < 60 ? lane - g * 30 : 0;
      const int qrow = j * 80 + v * 20 + g * 10;
      const bool isq = ch < 24;
      const char* src = isq ? (const char*)wq + (size_t)qrow * (NH * DH * 2) + h * ROWB + ch * 16
                            : (const char*)w1 + (size_t)qrow * (FF * 2) + h * 96 + (ch - 24) * 16;
      const int rstride = isq ? NH * DH * 2 : FF * 2;
      u32x4 xa[4]; unsigned xb[4];
#pragma unroll
      for (int bq = 0; bq < 4; ++bq) {
        xa[bq] = __builtin_amdgcn_raw_buffer_load_b128(rsX, qrow * 2, bq * DM * 2, 0);
        xb[bq] = __builtin_amdgcn_raw_buffer_load_b32(rsX, qrow * 2 + 16, bq * DM * 2, 0);
      }
      u32x4 wr[10];
#pragma unroll
      for (int i = 0; i < 10; ++i) wr[i] = __builtin_nontemporal_load(reinterpret_cast<const u32x4*>(src + i * rstride));
      SCHED_FENCE();
      BAR();
      float qa[4][8];
#pragma unroll
      for (int bq = 0; bq < 4; ++bq)
#pragma unroll
        for (int k = 0; k < 8; ++k) qa[bq][k] = 0.f;
#pragma unroll
      for (int p = 0; p < 5; ++p)
#pragma unroll
        for (int k = 0; k < 4; ++k) {
          const unsigned lo = __builtin_amdgcn_perm(wr[2 * p + 1][k], wr[2 * p][k], 0x05040100u);
          const unsigned hi = __builtin_amdgcn_perm(wr[2 * p + 1][k], wr[2 * p][k], 0x07060302u);
#pragma unroll
          for (int bq = 0; bq < 4; ++bq) {
            const unsigned xp = p < 4 ? xa[bq][p & 3] : xb[bq];
            qa[bq][2 * k] = dot2(lo, xp, qa[bq][2 * k]);
            qa[bq][2 * k + 1] = dot2(hi, xp, qa[bq][2 * k + 1]);
          }
        }
      if (lane < 60) {
        float* dst = &sm.zred[(v * 2 + g) * (NB * 2 * QP) + ch * 8];
#pragma unroll
        for (int bq = 0; bq < 4; ++bq) {
          *reinterpret_cast<f32x4*>(dst + bq * 2 * QP) = f32x4{qa[bq][0], qa[bq][1], qa[bq][2], qa[bq][3]};
          *reinterpret_cast<f32x4*>(dst + bq * 2 * QP + 4) = f32x4{qa[bq][4], qa[bq][5], qa[bq][6], qa[bq][7]};
        }
      }
    }
    lds_signal(&sm.cnt[0], lane);
    lds_wait(&sm.cnt[0], 4u);
    STAMP(6);
#pragma unroll
    for (int i = 0; i < 2; ++i) {
      const int e = i * 256 + at;
      if (e < NB * QP) {
        const int bq = e / QP, dp = e - bq * QP;
        float q0 = 0.f, q1 = 0.f;
#pragma unroll
        for (int w = 0; w < 8; ++w) { const f32x2 t = *reinterpret_cast<const f32x2*>(&sm.zred[w * (NB * 2 * QP) + e * 2]); q0 += t[0]; q1 += t[1]; }
        const unsigned u0 = __float_as_uint(q0), u1 = __float_as_uint(q1);
        __builtin_amdgcn_raw_buffer_store_b128(u32x4{u0, u1, tag, (u0 ^ u1) + tag * 0x9e3779b1u}, rsW, WS_Q + (((h * NB + bq) * 8 + j) * QP + dp) * 16, 0, AUX_SC1);
      }
    }
    {
      const int t = at & 127;
      const bool act = t < 96;
      const int gb = v < 2 ? b : (act ? t / 24 : 0), gp = v < 2 ? (act ? t : 0) : 96 + (act ? t % 24 : 0);
      const int go = WS_Q + ((h * NB + gb) * 8 * QP + gp) * 16;
      float q0, q1;
      bool okq = true;
      __builtin_amdgcn_s_sleep(POLLDELAY);
      for (unsigned spin = 0;; ++spin) {
        asm volatile("" ::: "memory");
        u32x4 g[8];
#pragma unroll
        for (int w = 0; w < 8; ++w) g[w] = __builtin_amdgcn_raw_buffer_load_b128(rsW, go + w * QP * 16, 0, AUX_SC1);
        bool ok = true;
        q0 = 0.f; q1 = 0.f;
#pragma unroll
        for (int w = 0; w < 8; ++w) {
          ok = ok && g[w][2] == tag && g[w][3] == (g[w][0] ^ g[w][1]) + tag * 0x9e3779b1u;
          q0 += __uint_as_float(g[w][0]); q1 += __uint_as_float(g[w][1]);
        }
        if (__all(ok)) break;
        if (spin > SPIN_CAP) { okq = false; break; }
      }
      if (v < 2) {
        if (act) sm.qpk[t] = okq ? cvtpk(q0, q1) : 0x7fc07fc0u;
        lds_signal(&sm.cnt[1], lane);
      } else if (act) {
        const float h0 = rbf(q0), h1 = rbf(q1);
        const float s0 = rbf(h0 / (1.f + __expf(-h0))), s1 = rbf(h1 / (1.f + __expf(-h1)));
        *reinterpret_cast<f32x2*>(&sm.hid[t * 2]) = okq ? f32x2{s0, s1} : f32x2{__builtin_nanf(""), __builtin_nanf("")};
      }
    }
    STAMP(1);
    {
      const unsigned* x32 = reinterpret_cast<const unsigned*>(x);
      const unsigned* g32 = reinterpret_cast<const unsigned*>(gw);
      unsigned xv[5], gv[5];
#pragma unroll
      for (int i = 0; i < 5; ++i) { const int e = i * 64 + lane, bb = e / 80, dd = e - bb * 80; xv[i] = x32[bb * (DM / 2) + row0 / 2 + dd]; gv[i] = g32[row0 / 2 + dd]; }
      u32x4 wv[10];
      const rsrc_t rsO = mk_rsrc(wo, NH * DH * DM * 2), rs2 = mk_rsrc(w2, FF * DM * 2);
#pragma unroll
      for (int i = 0; i < 10; ++i) {
        const int e = i * 256 + at, row = e / 10, ch = e - row * 10;
        const bool isO = i < 7 || (i == 7 && v < 2);
        const int off = e < KTOT * 10 ? ((isO ? h * DH + row : h * 48 + row - DH) * DM + j * NCOL + ch * 8) * 2 : 0x7ffffff0;
        wv[i] = __builtin_amdgcn_raw_buffer_load_b128(isO ? rsO : rs2, off, 0, AUX_NT);
      }
      SCHED_FENCE();
      if (at == 0) sm.tag[0] = tag;
      float gp[4] = {0.f, 0.f, 0.f, 0.f};
#pragma unroll
      for (int i = 0; i < 5; ++i) {
        const int e = i * 64 + lane, bb = e / 80;
        const float pr = rbf(bflo(xv[i]) * bflo(gv[i])) + rbf(bfhi(xv[i]) * bfhi(gv[i]));
#pragma unroll
        for (int bq = 0; bq < 4; ++bq) gp[bq] += (bb == bq) ? pr : 0.f;
      }
#pragma unroll
      for (int bq = 0; bq < 4; ++bq) { const float s = wave_sum(gp[bq]); if (lane == 0) sm.gred[v * 4 + bq] = s; }
      STAMP(3);
      lds_wait(&sm.cnt[2], (unsigned)NSW);
#pragma unroll
      for (int i = 0; i < 10; ++i) { const int e = i * 256 + at; if (e < KTOT * 10) *reinterpret_cast<u32x4*>(&sm.wsl[e * 8]) = wv[i]; }
      STAMP(5);
    }
    STAMP(2);
  }
  BAR();
  sec_aux = false;
  STAMP(4);
  const unsigned tag = sm.tag[0];
  if (tid < 194) {
    float M = sm.ml[0];
#pragma unroll
    for (int w = 1; w < NSW; ++w) M = fmaxf(M, sm.ml[2 * w]);
    float o = 0.f;
#pragma unroll
    for (int w = 0; w < NSW; ++w) {
      const float e = __builtin_amdgcn_exp2f(sm.ml[2 * w] - M);
      o = fmaf(e, tid < DH ? sm.mrg[w * DH + tid] : sm.ml[2 * w + 1], o);
    }
    const float on = __shfl_down(o, 1);
    if (!(tid & 1)) st_granule(rsW, WS_S1 + wg * S1_STRIDE + tid * 8, tag, tid == 192 ? M : o, on);
  }
  float gate;
  {
    const int bb = tid < 768 ? tid / DH : 0;
    float gs = (sm.gred[bb] + sm.gred[4 + bb]) + (sm.gred[8 + bb] + sm.gred[12 + bb]);
    gs = rbf(gs);
    gate = rbf(1.f / (1.f + __expf(-gs)));
    const int d = tid - bb * DH;
    if (d < 48) sm.act[(DH + d) * NB + bb] = sm.hid[bb * 48 + d] * rbf(1.f - gate);
  }
  bool okall = true;
  {
    const int bb = tid / DH, d = tid - bb * DH;
    const int p0 = xcd + 8 * ((h & 3) * 8 + bb * 2), p1 = p0 + 8;
    const int o0 = WS_S1 + p0 * S1_STRIDE, o1 = WS_S1 + p1 * S1_STRIDE;
    u32x4 g0, g1, m0, m1;
    for (unsigned spin = 0;; ++spin) {
      asm volatile("" ::: "memory");
      g0 = __builtin_amdgcn_raw_buffer_load_b128(rsW, o0 + (d >> 1) * 16, 0, AUX_SC1);
      g1 = __builtin_amdgcn_raw_buffer_load_b128(rsW, o1 + (d >> 1) * 16, 0, AUX_SC1);
      m0 = __builtin_amdgcn_raw_buffer_load_b128(rsW, o0 + 192 * 8, 0, AUX_SC1);
      m1 = __builtin_amdgcn_raw_buffer_load_b128(rsW, o1 + 192 * 8, 0, AUX_SC1);
      const bool ok = granule_ok(g0, tag) && granule_ok(g1, tag) && granule_ok(m0, tag) && granule_ok(m1, tag);
      if (__all(ok)) break;
      if (spin > SPIN_CAP) { okall = false; break; }
      __builtin_amdgcn_s_sleep(1);
    }
    const float M0 = __uint_as_float(m0[0]), L0 = __uint_as_float(m0[1]), M1 = __uint_as_float(m1[0]), L1 = __uint_as_float(m1[1]);
    const float M = fmaxf(M0, M1), e0 = __builtin_amdgcn_exp2f(M0 - M), e1 = __builtin_amdgcn_exp2f(M1 - M);
    const float o0v = __uint_as_float((d & 1) ? g0[1] : g0[0]), o1v = __uint_as_float((d & 1) ? g1[1] : g1[0]);
    const float o = (e0 * o0v + e1 * o1v) / (e0 * L0 + e1 * L1);
    sm.act[d * NB + bb] = okall ? rbf(o) * gate : __builtin_nanf("");
  }
  STAMP(5);
  BAR();
  if (tid < 600) {
    const int kg = tid / 20, cq = tid - kg * 20;
    float z[4][4];
#pragma unroll
    for (int i = 0; i < 4; ++i)
#pragma unroll
      for (int k = 0; k < 4; ++k) z[i][k] = 0.f;
#pragma unroll
    for (int rr = 0; rr < 8; ++rr) {
      const int row = kg * 8 + rr;
      const u32x2 wv = *reinterpret_cast<const u32x2*>(&sm.wsl[row * NCOL + cq * 4]);
      const f32x4 a = *reinterpret_cast<const f32x4*>(&sm.act[row * NB]);
      const float w0 = bflo(wv[0]), w1f = bfhi(wv[0]), w2f = bflo(wv[1]), w3 = bfhi(wv[1]);
#pragma unroll
      for (int i = 0; i < 4; ++i) { z[i][0] = fmaf(a[i], w0, z[i][0]); z[i][1] = fmaf(a[i], w1f, z[i][1]); z[i][2] = fmaf(a[i], w2f, z[i][2]); z[i][3] = fmaf(a[i], w3, z[i][3]); }
    }
#pragma unroll
    for (int i = 0; i < 4; ++i) *reinterpret_cast<f32x4*>(&sm.zred[kg * 320 + i * NCOL + cq * 4]) = f32x4{z[i][0], z[i][1], z[i][2], z[i][3]};
  }
  BAR();
  if (tid < 320) {
    float z = 0.f;
#pragma unroll
    for (int kg = 0; kg < 30; ++kg) z += sm.zred[kg * 320 + tid];
    const int bb = tid / 80, col = tid - bb * 80, f = (j * 4 + bb) * 8 + col / 10;
    const float zn = __shfl_down(z, 1);
    if (!(tid & 1)) st_granule(rsW, WS_S2 + f * S2_STRIDE + (h * 10 + col % 10) * 8, tag, z, zn);
    u32x4 g;
    bool ok2 = true;
    for (unsigned spin = 0;; ++spin) {
      asm volatile("" ::: "memory");
      g = __builtin_amdgcn_raw_buffer_load_b128(rsW, WS_S2 + wg * S2_STRIDE + (tid >> 1) * 16, 0, AUX_SC1);
      if (__all(granule_ok(g, tag))) break;
      if (spin > SPIN_CAP) { ok2 = false; break; }
      __builtin_amdgcn_s_sleep(1);
    }
    sm.fin[tid] = ok2 ? __uint_as_float((tid & 1) ? g[1] : g[0]) : __builtin_nanf("");
  }
  STAMP(6);
  BAR();
  if (tid < 10) {
    float z = 0.f;
#pragma unroll
    for (int hh = 0; hh < NH; ++hh) z += sm.fin[hh * 10 + tid];
    const int jj = wg >> 5, bb = (wg >> 3) & 3, sub = wg & 7;
    out[bb * DM + jj * NCOL + sub * 10 + tid] = (unsigned short)(cvtpk(z, z) & 0xffffu);
    if (wg == 0 && tid == 0) __hip_atomic_store((GAS unsigned*)(ws + WS_EPOCH), tag, RLX_AGENT);
  }
  STAMP(7);
  asm volatile(".Lkcode_b:");
}

}

extern "C" void kernel_launch(void* const* d_in, const int* in_sizes, int n_in, void* d_out, int out_size, void* d_ws,
                              size_t ws_size, hipStream_t stream) {
  (void)in_sizes; (void)n_in; (void)out_size; (void)ws_size;
  const unsigned short* ax = (const unsigned short*)d_in[0];
  const unsigned short* kc = (const unsigned short*)d_in[1];
  const unsigned short* vc = (const unsigned short*)d_in[2];
  const unsigned short* wq = (const unsigned short*)d_in[3];
  const unsigned short* wo = (const unsigned short*)d_in[4];
  const unsigned short* w1 = (const unsigned short*)d_in[6];
  const unsigned short* w2 = (const unsigned short*)d_in[7];
  const unsigned short* gw = (const unsigned short*)d_in[8];
  fused_kernel<<<NWG, NTHR, 0, stream>>>(ax, kc, vc, wq, wo, w1, w2, gw, (unsigned short*)d_out, (unsigned char*)d_ws);
}
```

```cpp
#include <hip/hip_runtime.h>
#include <hip/hip_bf16.h>
#include <cstdint>

namespace {

constexpr int NB = 4, DM = 640, NH = 32, SK = 4096, DH = 192, FF = 1536;
constexpr int NWG = 256, NSW = 8, NAW = 4, NTHR = (NSW + NAW) * 64;
constexpr int ROWB = DH * 2;
constexpr int TILEB = 8 * ROWB;
constexpr int KEYS_WG = SK / 2;
constexpr int KEYS_W = KEYS_WG / NSW;
constexpr int POLLDELAY = 120;
constexpr int LT = 4;
constexpr int WIN = 8;
constexpr int KTOT = DH + 48;
constexpr int NCOL = 80;

constexpr int WS_EPOCH = 0;
constexpr int WS_S1 = 4096;
constexpr int S1_STRIDE = 256 * 8;
constexpr int WS_S2 = WS_S1 + NWG * S1_STRIDE;
constexpr int S2_STRIDE = 320 * 8;
constexpr int QP = (DH + 48) / 2;
constexpr int WS_Q = WS_S2 + NWG * S2_STRIDE;
constexpr int WS_BYTES = WS_Q + NH * NB * 8 * QP * 16;

typedef unsigned u32x4 __attribute__((ext_vector_type(4)));
typedef unsigned u32x2 __attribute__((ext_vector_type(2)));
typedef float f32x4 __attribute__((ext_vector_type(4)));
typedef float f32x2 __attribute__((ext_vector_type(2)));
typedef __bf16 bf16x2 __attribute__((ext_vector_type(2)));
typedef __amdgpu_buffer_rsrc_t rsrc_t;
#define GAS __attribute__((address_space(1)))
#define RLX_AGENT __ATOMIC_RELAXED, __HIP_MEMORY_SCOPE_AGENT
constexpr int AUX_NT = 2, AUX_SC1 = 16;
constexpr unsigned SPIN_CAP = 1u << 18;

__device__ __forceinline__ rsrc_t mk_rsrc(const void* p, unsigned bytes) {
  return __builtin_amdgcn_make_buffer_rsrc(const_cast<void*>(p), (short)0, (int)bytes, 0x00020000);
}
__device__ __forceinline__ float bflo(unsigned u) { return __builtin_bit_cast(float, u << 16); }
__device__ __forceinline__ float bfhi(unsigned u) { return __builtin_bit_cast(float, u & 0xffff0000u); }
__device__ __forceinline__ unsigned cvtpk(float lo, float hi) {
  unsigned r; asm("v_cvt_pk_bf16_f32 %0, %1, %2" : "=v"(r) : "v"(lo), "v"(hi)); return r;
}
__device__ __forceinline__ float rbf(float x) { return bflo(cvtpk(x, x)); }
__device__ __forceinline__ float dot2(unsigned k, unsigned q, float acc) {
  return __builtin_amdgcn_fdot2_f32_bf16(__builtin_bit_cast(bf16x2, k), __builtin_bit_cast(bf16x2, q), acc, false);
}
template <int CTRL> __device__ __forceinline__ float dpp(float x) {
  return __builtin_bit_cast(float, __builtin_amdgcn_mov_dpp(__builtin_bit_cast(int, x), CTRL, 0xf, 0xf, true));
}
constexpr int XOR1 = 0xB1, XOR2 = 0x4E, XOR7 = 0x141, ROR8 = 0x128;
__device__ __forceinline__ float swap32_add(float a, float b) {
  auto s = __builtin_amdgcn_permlane32_swap(__float_as_uint(a), __float_as_uint(b), false, false);
  return __uint_as_float(s[0]) + __uint_as_float(s[1]);
}
__device__ __forceinline__ float swap16_add(float a, float b) {
  auto s = __builtin_amdgcn_permlane16_swap(__float_as_uint(a), __float_as_uint(b), false, false);
  return __uint_as_float(s[0]) + __uint_as_float(s[1]);
}
__device__ __forceinline__ float wave_max(float x) {
  x = fmaxf(x, dpp<XOR1>(x)); x = fmaxf(x, dpp<XOR2>(x)); x = fmaxf(x, dpp<XOR7>(x)); x = fmaxf(x, dpp<ROR8>(x));
  auto s = __builtin_amdgcn_permlane16_swap(__float_as_uint(x), __float_as_uint(x), false, false);
  x = fmaxf(__uint_as_float(s[0]), __uint_as_float(s[1]));
  auto t = __builtin_amdgcn_permlane32_swap(__float_as_uint(x), __float_as_uint(x), false, false);
  return fmaxf(__uint_as_float(t[0]), __uint_as_float(t[1]));
}
__device__ __forceinline__ float wave_sum(float x) {
  x += dpp<XOR1>(x); x += dpp<XOR2>(x); x += dpp<XOR7>(x); x += dpp<ROR8>(x);
  x = swap16_add(x, x);
  return swap32_add(x, x);
}
template <int N> __device__ __forceinline__ void tr_reduce(float (&v)[N], int lane) {
#pragma unroll
  for (int i = 0; i < N / 2; ++i) v[i] = swap32_add(v[i], v[i + N / 2]);
#pragma unroll
  for (int i = 0; i < N / 4; ++i) v[i] = swap16_add(v[i], v[i + N / 4]);
  const bool b3 = (lane & 8) != 0;
#pragma unroll
  for (int i = 0; i < N / 8; ++i) {
    const float keep = b3 ? v[i + N / 8] : v[i], send = b3 ? v[i] : v[i + N / 8];
    v[i] = keep + dpp<ROR8>(send);
  }
}
#define BAR() asm volatile("s_waitcnt lgkmcnt(0)\n\ts_barrier" ::: "memory")
#define SCHED_FENCE() __builtin_amdgcn_sched_barrier(0)
__device__ __forceinline__ void lds_signal(unsigned* p, int lane) {
  asm volatile("s_waitcnt lgkmcnt(0)" ::: "memory");
  if (lane == 0) __hip_atomic_fetch_add(p, 1u, __ATOMIC_RELAXED, __HIP_MEMORY_SCOPE_WORKGROUP);
}
__device__ __forceinline__ void lds_wait(unsigned* p, unsigned n) {
  while (__builtin_amdgcn_readfirstlane(__hip_atomic_load(p, __ATOMIC_RELAXED, __HIP_MEMORY_SCOPE_WORKGROUP)) < n) __builtin_amdgcn_s_sleep(1);
  asm volatile("" ::: "memory");
}

template <int AUX> __device__ __forceinline__ void ld_tile(u32x4 (&t)[3], rsrc_t rs, int voff, int soff) {
  t[0] = __builtin_amdgcn_raw_buffer_load_b128(rs, voff, soff, AUX);
  t[1] = __builtin_amdgcn_raw_buffer_load_b128(rs, voff + 128, soff, AUX);
  t[2] = __builtin_amdgcn_raw_buffer_load_b128(rs, voff + 256, soff, AUX);
}
__device__ __forceinline__ void fma_tile(float (&acc)[24], const u32x4 (&t)[3], float p) {
#pragma unroll
  for (int s = 0; s < 3; ++s)
#pragma unroll
    for (int k = 0; k < 4; ++k) {
      acc[s * 8 + 2 * k] = fmaf(p, bflo(t[s][k]), acc[s * 8 + 2 * k]);
      acc[s * 8 + 2 * k + 1] = fmaf(p, bfhi(t[s][k]), acc[s * 8 + 2 * k + 1]);
    }
}
__device__ __forceinline__ void pin_acc(float (&a)[24]) {
  asm volatile("" : "+v"(a[0]), "+v"(a[1]), "+v"(a[2]), "+v"(a[3]), "+v"(a[4]), "+v"(a[5]), "+v"(a[6]), "+v"(a[7]),
               "+v"(a[8]), "+v"(a[9]), "+v"(a[10]), "+v"(a[11]), "+v"(a[12]), "+v"(a[13]), "+v"(a[14]), "+v"(a[15]),
               "+v"(a[16]), "+v"(a[17]), "+v"(a[18]), "+v"(a[19]), "+v"(a[20]), "+v"(a[21]), "+v"(a[22]), "+v"(a[23])
               :: "memory");
}
__device__ __forceinline__ void st_granule(rsrc_t rs, int off, unsigned tag, float v0, float v1) {
  const unsigned u0 = __float_as_uint(v0), u1 = __float_as_uint(v1);
  __builtin_amdgcn_raw_buffer_store_b128(u32x4{u0, u1, tag, (u0 ^ u1) + tag * 0x9e3779b1u}, rs, off, 0, AUX_SC1);
}
__device__ __forceinline__ bool granule_ok(const u32x4& g, unsigned tag) {
  return g[2] == tag && g[3] == (g[0] ^ g[1]) + tag * 0x9e3779b1u;
}

struct __attribute__((aligned(16))) Smem {
  float pbuf[NSW * 256];
  float mrg[NSW * DH];
  float ml[NSW * 2];
  unsigned qpk[DH / 2];
  float hid[NB * 48];
  float gred[NAW * NB];
  unsigned tag[4];
  unsigned cnt[4];
  float act[KTOT * NB];
  union {
    u32x4 kl[NSW * LT * 3 * 64];
    unsigned short wsl[KTOT * NCOL];
  };
  float zred[30 * NB * NCOL];
  float fin[320];
};


__global__ void __launch_bounds__(NTHR, 3)
fused_kernel(const unsigned short* __restrict__ x, const unsigned short* __restrict__ kc, const unsigned short* __restrict__ vc,
             const unsigned short* __restrict__ wq, const unsigned short* __restrict__ wo, const unsigned short* __restrict__ w1,
             const unsigned short* __restrict__ w2, const unsigned short* __restrict__ gw, unsigned short* __restrict__ out,
             unsigned char* __restrict__ ws) {
  __shared__ Smem sm;
  const int tid = threadIdx.x, lane = tid & 63, wave = __builtin_amdgcn_readfirstlane(tid >> 6);
  const int wg = blockIdx.x, xcd = wg & 7, idx = wg >> 3;
  const int h = xcd * 4 + (idx >> 3), j = idx & 7, b = j >> 1, sp = j & 1;
  const int r = lane >> 3, c = lane & 7;
  const rsrc_t rsW = mk_rsrc(ws, WS_BYTES);

  float acc[24];
#pragma unroll
  for (int i = 0; i < 24; ++i) acc[i] = 0.f;

  if (wave < NSW) {
    const rsrc_t rsK = mk_rsrc(kc, NB * NH * SK * ROWB), rsV = mk_rsrc(vc, NB * NH * SK * ROWB), rs0 = mk_rsrc(vc, 0);
    const int voff = r * ROWB + c * 16;
    const int base = ((b * NH + h) * SK + sp * KEYS_WG) * ROWB + wave * TILEB;
    constexpr int TS = NSW * TILEB;
    u32x4 buf[WIN][3];
    if (tid < 4) sm.cnt[tid] = 0u;
    BAR();
    {
      const unsigned lbase = (unsigned)(size_t)(__attribute__((address_space(3))) void*)&sm.kl[wave * LT * 3 * 64];
#pragma unroll
      for (int t = 0; t < LT; ++t) {
#pragma unroll
        for (int s = 0; s < 3; ++s) {
          unsigned keep;
          asm volatile("s_mov_b32 %0, m0\n\ts_mov_b32 m0, %2\n\ts_nop 0\n\tglobal_load_lds_dwordx4 %1, %3 nt\n\ts_mov_b32 m0, %0"
                       : "=&s"(keep) : "v"((unsigned)(base + t * TS + voff + s * 128)), "s"(lbase + (t * 3 + s) * 1024), "s"(kc) : "memory");
        }
      }
    }
#pragma unroll
    for (int u = 0; u < WIN; ++u) {
      ld_tile<AUX_NT>(buf[u], rsK, voff, base + (LT + u) * TS);
      SCHED_FENCE();
    }
    lds_wait(&sm.cnt[1], 2u);
    u32x4 q[3];
#pragma unroll
    for (int s = 0; s < 3; ++s) q[s] = *reinterpret_cast<const u32x4*>(&sm.qpk[s * 32 + c * 4]);
    float* pw = &sm.pbuf[wave * 256 + r * 32];
    auto score = [&](const u32x4 (&t)[3]) -> float {
      float d0 = 0.f, d1 = 0.f, d2 = 0.f;
#pragma unroll
      for (int k = 0; k < 4; ++k) { d0 = dot2(t[0][k], q[0][k], d0); d1 = dot2(t[1][k], q[1][k], d1); d2 = dot2(t[2][k], q[2][k], d2); }
      return (d0 + d1) + d2;
    };
    auto rowsum = [&](float d) -> float { d += dpp<XOR1>(d); d += dpp<XOR2>(d); d += dpp<XOR7>(d); return d; };
    float sc = 0.f;
    asm volatile("s_waitcnt vmcnt(%0)" :: "n"(3 * WIN) : "memory");
#pragma unroll
    for (int t = 0; t < LT; ++t) {
      u32x4 kt[3];
#pragma unroll
      for (int s = 0; s < 3; ++s) kt[s] = sm.kl[((wave * LT + t) * 3 + s) * 64 + lane];
      const float d = rowsum(score(kt));
      sc = (c == t) ? d : sc;
    }
    lds_signal(&sm.cnt[2], lane);
#pragma unroll 1
    for (int it = 0; it < 3; ++it) {
      const rsrc_t rn = it < 2 ? rsK : rsV;
      const int na = base + (LT + 8 + it * 8) * TS, nb = it < 2 ? na : base - LT * TS;
#pragma unroll
      for (int u = 0; u < 8; ++u) {
        const float d0 = score(buf[u]);
        SCHED_FENCE();
        if (u < 4) ld_tile<AUX_NT>(buf[u], rsK, voff, na + u * TS);
        else ld_tile<AUX_NT>(buf[u], rn, voff, nb + u * TS);
        SCHED_FENCE();
        const float d = rowsum(d0);
        sc = (c == ((u + LT) & 7)) ? d : sc;
        if (u == 3) pw[it * 8 + c] = sc;
      }
    }
#pragma unroll
    for (int u = 0; u < 4; ++u) {
      const float d0 = score(buf[u]);
      SCHED_FENCE();
      ld_tile<AUX_NT>(buf[u], rsV, voff, base + (4 + u) * TS);
      SCHED_FENCE();
      const float d = rowsum(d0);
      sc = (c == ((u + LT) & 7)) ? d : sc;
    }
    pw[3 * 8 + c] = sc;
    constexpr float C2 = 0.07216878364870322f * 1.4426950408889634f;
    float s4[4], m = -INFINITY;
#pragma unroll
    for (int it = 0; it < 4; ++it) { s4[it] = pw[it * 8 + c] * C2; m = fmaxf(m, s4[it]); }
    m = wave_max(m);
    float l = 0.f;
#pragma unroll
    for (int it = 0; it < 4; ++it) { const float p = __builtin_amdgcn_exp2f(s4[it] - m); l += p; pw[it * 8 + c] = p; }
    l = wave_sum(l);
#pragma unroll 1
    for (int it = 0; it < 4; ++it) {
      const rsrc_t rn = it < 3 ? rsV : rs0;
      const int nb = base + (it + 1) * 8 * TS;
      const f32x4 pa = *reinterpret_cast<const f32x4*>(pw + it * 8), pb = *reinterpret_cast<const f32x4*>(pw + it * 8 + 4);
#pragma unroll
      for (int u = 0; u < 8; ++u) {
        fma_tile(acc, buf[(u + LT) & 7], u < 4 ? pa[u & 3] : pb[u & 3]);
        pin_acc(acc);
        SCHED_FENCE();
        ld_tile<AUX_NT>(buf[(u + LT) & 7], rn, voff, nb + u * TS);
        SCHED_FENCE();
      }
    }
    tr_reduce<24>(acc, lane);
    const int a0 = 3 * ((lane >> 3) & 1) + 6 * ((lane >> 4) & 1) + 12 * (lane >> 5);
#pragma unroll
    for (int i = 0; i < 3; ++i) { const int a = a0 + i; sm.mrg[wave * DH + (a >> 3) * 64 + c * 8 + (a & 7)] = acc[i]; }
    if (lane == 0) { sm.ml[wave * 2] = m; sm.ml[wave * 2 + 1] = l; }
  } else {
    __builtin_amdgcn_s_setprio(3);
    const int v = wave - NSW, row0 = v * 160, at = tid - NSW * 64;
    const unsigned tag = __hip_atomic_load((GAS unsigned*)(ws + WS_EPOCH), RLX_AGENT) + 1u;
    {
      const rsrc_t rsX = mk_rsrc(x, NB * DM * 2);
      const int g = lane >= 30 ? 1 : 0, ch = lane < 60 ? lane - g * 30 : 0;
      const int qrow = j * 80 + v * 20 + g * 10;
      const bool isq = ch < 24;
      const char* src = isq ? (const char*)wq + (size_t)qrow * (NH * DH * 2) + h * ROWB + ch * 16
                            : (const char*)w1 + (size_t)qrow * (FF * 2) + h * 96 + (ch - 24) * 16;
      const int rstride = isq ? NH * DH * 2 : FF * 2;
      u32x4 xa[4]; unsigned xb[4];
#pragma unroll
      for (int bq = 0; bq < 4; ++bq) {
        xa[bq] = __builtin_amdgcn_raw_buffer_load_b128(rsX, qrow * 2, bq * DM * 2, 0);
        xb[bq] = __builtin_amdgcn_raw_buffer_load_b32(rsX, qrow * 2 + 16, bq * DM * 2, 0);
      }
      u32x4 wr[10];
#pragma unroll
      for (int i = 0; i < 10; ++i) wr[i] = __builtin_nontemporal_load(reinterpret_cast<const u32x4*>(src + i * rstride));
      SCHED_FENCE();
      BAR();
      float qa[4][8];
#pragma unroll
      for (int bq = 0; bq < 4; ++bq)
#pragma unroll
        for (int k = 0; k < 8; ++k) qa[bq][k] = 0.f;
#pragma unroll
      for (int p = 0; p < 5; ++p)
#pragma unroll
        for (int k = 0; k < 4; ++k) {
          const unsigned lo = __builtin_amdgcn_perm(wr[2 * p + 1][k], wr[2 * p][k], 0x05040100u);
          const unsigned hi = __builtin_amdgcn_perm(wr[2 * p + 1][k], wr[2 * p][k], 0x07060302u);
#pragma unroll
          for (int bq = 0; bq < 4; ++bq) {
            const unsigned xp = p < 4 ? xa[bq][p & 3] : xb[bq];
            qa[bq][2 * k] = dot2(lo, xp, qa[bq][2 * k]);
            qa[bq][2 * k + 1] = dot2(hi, xp, qa[bq][2 * k + 1]);
          }
        }
      if (lane < 60) {
        float* dst = &sm.zred[(v * 2 + g) * (NB * 2 * QP) + ch * 8];
#pragma unroll
        for (int bq = 0; bq < 4; ++bq) {
          *reinterpret_cast<f32x4*>(dst + bq * 2 * QP) = f32x4{qa[bq][0], qa[bq][1], qa[bq][2], qa[bq][3]};
          *reinterpret_cast<f32x4*>(dst + bq * 2 * QP + 4) = f32x4{qa[bq][4], qa[bq][5], qa[bq][6], qa[bq][7]};
        }
      }
    }
    lds_signal(&sm.cnt[0], lane);
    lds_wait(&sm.cnt[0], 4u);
#pragma unroll
    for (int i = 0; i < 2; ++i) {
      const int e = i * 256 + at;
      if (e < NB * QP) {
        const int bq = e / QP, dp = e - bq * QP;
        float q0 = 0.f, q1 = 0.f;
#pragma unroll
        for (int w = 0; w < 8; ++w) { const f32x2 t = *reinterpret_cast<const f32x2*>(&sm.zred[w * (NB * 2 * QP) + e * 2]); q0 += t[0]; q1 += t[1]; }
        const unsigned u0 = __float_as_uint(q0), u1 = __float_as_uint(q1);
        __builtin_amdgcn_raw_buffer_store_b128(u32x4{u0, u1, tag, (u0 ^ u1) + tag * 0x9e3779b1u}, rsW, WS_Q + (((h * NB + bq) * 8 + j) * QP + dp) * 16, 0, AUX_SC1);
      }
    }
    {
      const int t = at & 127;
      const bool act = t < 96;
      const int gb = v < 2 ? b : (act ? t / 24 : 0), gp = v < 2 ? (act ? t : 0) : 96 + (act ? t % 24 : 0);
      const int go = WS_Q + ((h * NB + gb) * 8 * QP + gp) * 16;
      float q0, q1;
      bool okq = true;
      __builtin_amdgcn_s_sleep(POLLDELAY);
      for (unsigned spin = 0;; ++spin) {
        asm volatile("" ::: "memory");
        u32x4 g[8];
#pragma unroll
        for (int w = 0; w < 8; ++w) g[w] = __builtin_amdgcn_raw_buffer_load_b128(rsW, go + w * QP * 16, 0, AUX_SC1);
        bool ok = true;
        q0 = 0.f; q1 = 0.f;
#pragma unroll
        for (int w = 0; w < 8; ++w) {
          ok = ok && g[w][2] == tag && g[w][3] == (g[w][0] ^ g[w][1]) + tag * 0x9e3779b1u;
          q0 += __uint_as_float(g[w][0]); q1 += __uint_as_float(g[w][1]);
        }
        if (__all(ok)) break;
        if (spin > SPIN_CAP) { okq = false; break; }
      }
      if (v < 2) {
        if (act) sm.qpk[t] = okq ? cvtpk(q0, q1) : 0x7fc07fc0u;
        lds_signal(&sm.cnt[1], lane);
      } else if (act) {
        const float h0 = rbf(q0), h1 = rbf(q1);
        const float s0 = rbf(h0 / (1.f + __expf(-h0))), s1 = rbf(h1 / (1.f + __expf(-h1)));
        *reinterpret_cast<f32x2*>(&sm.hid[t * 2]) = okq ? f32x2{s0, s1} : f32x2{__builtin_nanf(""), __builtin_nanf("")};
      }
    }
    {
      const unsigned* x32 = reinterpret_cast<const unsigned*>(x);
      const unsigned* g32 = reinterpret_cast<const unsigned*>(gw);
      unsigned xv[5], gv[5];
#pragma unroll
      for (int i = 0; i < 5; ++i) { const int e = i * 64 + lane, bb = e / 80, dd = e - bb * 80; xv[i] = x32[bb * (DM / 2) + row0 / 2 + dd]; gv[i] = g32[row0 / 2 + dd]; }
      u32x4 wv[10];
      const rsrc_t rsO = mk_rsrc(wo, NH * DH * DM * 2), rs2 = mk_rsrc(w2, FF * DM * 2);
#pragma unroll
      for (int i = 0; i < 10; ++i) {
        const int e = i * 256 + at, row = e / 10, ch = e - row * 10;
        const bool isO = i < 7 || (i == 7 && v < 2);
        const int off = e < KTOT * 10 ? ((isO ? h * DH + row : h * 48 + row - DH) * DM + j * NCOL + ch * 8) * 2 : 0x7ffffff0;
        wv[i] = __builtin_amdgcn_raw_buffer_load_b128(isO ? rsO : rs2, off, 0, AUX_NT);
      }
      SCHED_FENCE();
      if (at == 0) sm.tag[0] = tag;
      float gp[4] = {0.f, 0.f, 0.f, 0.f};
#pragma unroll
      for (int i = 0; i < 5; ++i) {
        const int e = i * 64 + lane, bb = e / 80;
        const float pr = rbf(bflo(xv[i]) * bflo(gv[i])) + rbf(bfhi(xv[i]) * bfhi(gv[i]));
#pragma unroll
        for (int bq = 0; bq < 4; ++bq) gp[bq] += (bb == bq) ? pr : 0.f;
      }
#pragma unroll
      for (int bq = 0; bq < 4; ++bq) { const float s = wave_sum(gp[bq]); if (lane == 0) sm.gred[v * 4 + bq] = s; }
      lds_wait(&sm.cnt[2], (unsigned)NSW);
#pragma unroll
      for (int i = 0; i < 10; ++i) { const int e = i * 256 + at; if (e < KTOT * 10) *reinterpret_cast<u32x4*>(&sm.wsl[e * 8]) = wv[i]; }
    }
  }
  BAR();
  const unsigned tag = sm.tag[0];
  if (tid < 194) {
    float M = sm.ml[0];
#pragma unroll
    for (int w = 1; w < NSW; ++w) M = fmaxf(M, sm.ml[2 * w]);
    float o = 0.f;
#pragma unroll
    for (int w = 0; w < NSW; ++w) {
      const float e = __builtin_amdgcn_exp2f(sm.ml[2 * w] - M);
      o = fmaf(e, tid < DH ? sm.mrg[w * DH + tid] : sm.ml[2 * w + 1], o);
    }
    const float on = __shfl_down(o, 1);
    if (!(tid & 1)) st_granule(rsW, WS_S1 + wg * S1_STRIDE + tid * 8, tag, tid == 192 ? M : o, on);
  }
  float gate;
  {
    const int bb = tid < 768 ? tid / DH : 0;
    float gs = (sm.gred[bb] + sm.gred[4 + bb]) + (sm.gred[8 + bb] + sm.gred[12 + bb]);
    gs = rbf(gs);
    gate = rbf(1.f / (1.f + __expf(-gs)));
    const int d = tid - bb * DH;
    if (d < 48) sm.act[(DH + d) * NB + bb] = sm.hid[bb * 48 + d] * rbf(1.f - gate);
  }
  bool okall = true;
  {
    const int bb = tid / DH, d = tid - bb * DH;
    const int p0 = xcd + 8 * ((h & 3) * 8 + bb * 2), p1 = p0 + 8;
    const int o0 = WS_S1 + p0 * S1_STRIDE, o1 = WS_S1 + p1 * S1_STRIDE;
    u32x4 g0, g1, m0, m1;
    for (unsigned spin = 0;; ++spin) {
      asm volatile("" ::: "memory");
      g0 = __builtin_amdgcn_raw_buffer_load_b128(rsW, o0 + (d >> 1) * 16, 0, AUX_SC1);
      g1 = __builtin_amdgcn_raw_buffer_load_b128(rsW, o1 + (d >> 1) * 16, 0, AUX_SC1);
      m0 = __builtin_amdgcn_raw_buffer_load_b128(rsW, o0 + 192 * 8, 0, AUX_SC1);
      m1 = __builtin_amdgcn_raw_buffer_load_b128(rsW, o1 + 192 * 8, 0, AUX_SC1);
      const bool ok = granule_ok(g0, tag) && granule_ok(g1, tag) && granule_ok(m0, tag) && granule_ok(m1, tag);
      if (__all(ok)) break;
      if (spin > SPIN_CAP) { okall = false; break; }
      __builtin_amdgcn_s_sleep(1);
    }
    const float M0 = __uint_as_float(m0[0]), L0 = __uint_as_float(m0[1]), M1 = __uint_as_float(m1[0]), L1 = __uint_as_float(m1[1]);
    const float M = fmaxf(M0, M1), e0 = __builtin_amdgcn_exp2f(M0 - M), e1 = __builtin_amdgcn_exp2f(M1 - M);
    const float o0v = __uint_as_float((d & 1) ? g0[1] : g0[0]), o1v = __uint_as_float((d & 1) ? g1[1] : g1[0]);
    const float o = (e0 * o0v + e1 * o1v) / (e0 * L0 + e1 * L1);
    sm.act[d * NB + bb] = okall ? rbf(o) * gate : __builtin_nanf("");
  }
  BAR();
  if (tid < 600) {
    const int kg = tid / 20, cq = tid - kg * 20;
    float z[4][4];
#pragma unroll
    for (int i = 0; i < 4; ++i)
#pragma unroll
      for (int k = 0; k < 4; ++k) z[i][k] = 0.f;
#pragma unroll
    for (int rr = 0; rr < 8; ++rr) {
      const int row = kg * 8 + rr;
      const u32x2 wv = *reinterpret_cast<const u32x2*>(&sm.wsl[row * NCOL + cq * 4]);
      const f32x4 a = *reinterpret_cast<const f32x4*>(&sm.act[row * NB]);
      const float w0 = bflo(wv[0]), w1f = bfhi(wv[0]), w2f = bflo(wv[1]), w3 = bfhi(wv[1]);
#pragma unroll
      for (int i = 0; i < 4; ++i) { z[i][0] = fmaf(a[i], w0, z[i][0]); z[i][1] = fmaf(a[i], w1f, z[i][1]); z[i][2] = fmaf(a[i], w2f, z[i][2]); z[i][3] = fmaf(a[i], w3, z[i][3]); }
    }
#pragma unroll
    for (int i = 0; i < 4; ++i) *reinterpret_cast<f32x4*>(&sm.zred[kg * 320 + i * NCOL + cq * 4]) = f32x4{z[i][0], z[i][1], z[i][2], z[i][3]};
  }
  BAR();
  if (tid < 320) {
    float z = 0.f;
#pragma unroll
    for (int kg = 0; kg < 30; ++kg) z += sm.zred[kg * 320 + tid];
    const int bb = tid / 80, col = tid - bb * 80, f = (j * 4 + bb) * 8 + col / 10;
    const float zn = __shfl_down(z, 1);
    if (!(tid & 1)) st_granule(rsW, WS_S2 + f * S2_STRIDE + (h * 10 + col % 10) * 8, tag, z, zn);
    u32x4 g;
    bool ok2 = true;
    for (unsigned spin = 0;; ++spin) {
      asm volatile("" ::: "memory");
      g = __builtin_amdgcn_raw_buffer_load_b128(rsW, WS_S2 + wg * S2_STRIDE + (tid >> 1) * 16, 0, AUX_SC1);
      if (__all(granule_ok(g, tag))) break;
      if (spin > SPIN_CAP) { ok2 = false; break; }
      __builtin_amdgcn_s_sleep(1);
    }
    sm.fin[tid] = ok2 ? __uint_as_float((tid & 1) ? g[1] : g[0]) : __builtin_nanf("");
  }
  BAR();
  if (tid < 10) {
    float z = 0.f;
#pragma unroll
    for (int hh = 0; hh < NH; ++hh) z += sm.fin[hh * 10 + tid];
    const int jj = wg >> 5, bb = (wg >> 3) & 3, sub = wg & 7;
    __builtin_amdgcn_raw_buffer_store_b16((unsigned short)(cvtpk(z, z) & 0xffffu), mk_rsrc(out, NB * DM * 2), (bb * DM + jj * NCOL + sub * 10 + tid) * 2, 0, AUX_SC1);
    if (wg == 0 && tid == 0) __hip_atomic_store((GAS unsigned*)(ws + WS_EPOCH), tag, RLX_AGENT);
  }
}

}

extern "C" void kernel_launch(void* const* d_in, const int* in_sizes, int n_in, void* d_out, int out_size, void* d_ws,
                              size_t ws_size, hipStream_t stream) {
  (void)in_sizes; (void)n_in; (void)out_size; (void)ws_size;
  const unsigned short* ax = (const unsigned short*)d_in[0];
  const unsigned short* kc = (const unsigned short*)d_in[1];
  const unsigned short* vc = (const unsigned short*)d_in[2];
  const unsigned short* wq = (const unsigned short*)d_in[3];
  const unsigned short* wo = (const unsigned short*)d_in[4];
  const unsigned short* w1 = (const unsigned short*)d_in[6];
  const unsigned short* w2 = (const unsigned short*)d_in[7];
  const unsigned short* gw = (const unsigned short*)d_in[8];
  fused_kernel<<<NWG, NTHR, 0, stream>>>(ax, kc, vc, wq, wo, w1, w2, gw, (unsigned short*)d_out, (unsigned char*)d_ws);
}
```
